```python
import torch

def _softmax(x, axis=-1):
    return torch.softmax(x, dim=axis)
def _gelu(x):
    return torch.nn.functional.gelu(x, approximate="tanh")
def _silu(x):
    return torch.nn.functional.silu(x)
def _sigmoid(x):
    return torch.sigmoid(x)
def _layernorm(x, axis=-1, eps=1e-5):
    m = x.mean(dim=axis, keepdim=True)
    v = x.var(dim=axis, keepdim=True, unbiased=False)
    return (x - m) / torch.sqrt(v + eps)
def _scatter_add(tgt, idx, src, axis=0):
    return tgt.clone().index_add_(axis, idx, src)
def _segment_sum(src, seg, n):
    out = torch.zeros((n,) + src.shape[1:], dtype=src.dtype)
    return out.index_add_(0, seg, src)
def _pad(x, pw):
    flat = tuple(v for pair in reversed(pw) for v in pair)
    return torch.nn.functional.pad(x, flat)

kernel_name = 'sequential_preLN__sliding_local_dilated__glu_gated_geglu__prompt'

def setup_inputs():

    _w = (lambda a: (lambda t: t.to(torch.bfloat16) if t.is_floating_point() else t)(torch.as_tensor(a)))
    def _A_setup():
        B, S, D = 1, 2048, 512
        return {
            "x": _w((lambda *s: torch.randn(*s, dtype=torch.bfloat16) * 0.05)(B, S, D)),
            "wqkv": _w((lambda *s: torch.randn(*s, dtype=torch.bfloat16) * 0.05)(D, 3 * D)),
            "wo": _w((lambda *s: torch.randn(*s, dtype=torch.bfloat16) * 0.05)(D, D)),
        }
    _A_in = _A_setup()

    def _B_setup():
        B, S, Dm, Df = 1, 2048, 512, 1376
        return {
            "x": _w((lambda *s: torch.randn(*s, dtype=torch.bfloat16) * 0.05)(B, S, Dm)),
            "w_gate": _w((lambda *s: torch.randn(*s, dtype=torch.bfloat16) * 0.8)(Dm, Df) * (Dm ** -0.5)),
            "w_up": _w((lambda *s: torch.randn(*s, dtype=torch.bfloat16) * 0.8)(Dm, Df) * (Dm ** -0.5)),
            "w_down": _w((lambda *s: torch.randn(*s, dtype=torch.bfloat16) * 0.8)(Df, Dm) * (Df ** -0.5)),
        }
    _B_in = _B_setup()

    return {
        "A_x": _A_in["x"],
        "A_wqkv": _A_in["wqkv"],
        "A_wo": _A_in["wo"],
        "B_x": _B_in["x"],
        "B_w_gate": _B_in["w_gate"],
        "B_w_up": _B_in["w_up"],
        "B_w_down": _B_in["w_down"],
    }

def reference(A_x, A_wqkv, A_wo, B_x, B_w_gate, B_w_up, B_w_down):
    def _A_ref(x, wqkv, wo):
        B, S, D = x.shape
        H = 32; Dh = D // H
        W, dil = 8, 2
        qkv = (lambda x, *d: x.permute(*d))((x @ wqkv).reshape(B, S, 3, H, Dh), 2, 0, 3, 1, 4)
        q, k, v = qkv[0], qkv[1], qkv[2]
        scale = Dh ** -0.5
        pos = torch.arange(S)
        off = torch.arange(W) * dil
        idx = pos[:, None] - off[None, :]
        mask = idx >= 0
        idx_c = torch.where(mask, idx, 0).reshape(-1)
        kg = k[:, :, idx_c, :].reshape(B, H, S, W, Dh)
        vg = v[:, :, idx_c, :].reshape(B, H, S, W, Dh)
        logits = torch.einsum("bhsd,bhswd->bhsw", q, kg) * scale
        logits = torch.where(mask[None, None, :, :], logits, -1e9)
        attn = _softmax(logits, axis=-1)
        o = torch.einsum("bhsw,bhswd->bhsd", attn, vg)
        return (lambda x, *d: x.permute(*d))(o, 0, 2, 1, 3).reshape(B, S, D) @ wo

    def _B_ref(x, w_gate, w_up, w_down):
        g = torch.einsum("bsd,df->bsf", x, w_gate)
        u = torch.einsum("bsd,df->bsf", x, w_up)
        h = _gelu(g) * u
        return torch.einsum("bsf,fd->bsd", h, w_down)

    x = A_x
    h = x + _A_ref(_layernorm(x, axis=-1), A_wqkv, A_wo)
    h2 = h + _B_ref(_layernorm(h, axis=-1), B_w_gate, B_w_up, B_w_down)
    return h2
```

```cpp
#include <hip/hip_runtime.h>
#include <hip/hip_bf16.h>
typedef unsigned short u16;
__device__ inline float b2f(u16 b){ return __uint_as_float(((unsigned)b)<<16); }
__device__ inline u16 f2b(float f){ unsigned u=__float_as_uint(f); u += 0x7fffu + ((u>>16)&1u); return (u16)(u>>16); }
__global__ void k_ln(const u16* x, u16* y){
  int row=blockIdx.x; int l=threadIdx.x; float v[8]; float s=0;
  for(int i=0;i<8;i++){ v[i]=b2f(x[row*512+l*8+i]); s+=v[i]; }
  for(int m=32;m>=1;m>>=1) s+=__shfl_xor(s,m);
  float mean=s/512.f; float q=0; for(int i=0;i<8;i++){ float d=v[i]-mean; q+=d*d; }
  for(int m=32;m>=1;m>>=1) q+=__shfl_xor(q,m);
  float r=rsqrtf(q/512.f+1e-5f);
  for(int i=0;i<8;i++) y[row*512+l*8+i]=f2b((v[i]-mean)*r);
}
__global__ void k_gemm(const u16* A,const u16* B,u16* C,const u16* res,int M,int N,int K){
  int n=blockIdx.x*64+threadIdx.x; int m=blockIdx.y; if(n>=N) return; float acc=0;
  for(int k=0;k<K;k++) acc+=b2f(A[m*K+k])*b2f(B[k*N+n]);
  if(res) acc = b2f(f2b(acc)) + b2f(res[m*N+n]);
  C[m*N+n]=f2b(acc);
}
__global__ void k_attn(const u16* qkv,u16* o){
  int t=blockIdx.x; int h=threadIdx.x; if(h>=32) return; float lg[8]; float mx=-1e30f;
  for(int w=0;w<8;w++){ int j=t-2*w; float s=0; if(j>=0){ for(int d=0;d<16;d++) s+=b2f(qkv[t*1536+h*16+d])*b2f(qkv[j*1536+512+h*16+d]); s*=0.25f;} else s=-1e9f; lg[w]=s; mx=fmaxf(mx,s);}
  float sum=0; for(int w=0;w<8;w++){ lg[w]=__expf(lg[w]-mx); sum+=lg[w]; }
  for(int d=0;d<16;d++){ float a=0; for(int w=0;w<8;w++){ int j=t-2*w; if(j<0) j=0; a+=lg[w]/sum*b2f(qkv[j*1536+1024+h*16+d]); } o[t*512+h*16+d]=f2b(a);}
}
__global__ void k_geglu(const u16* g,const u16* u,u16* h,int n){ int i=blockIdx.x*256+threadIdx.x; if(i<n){ float x=b2f(g[i]); float t=tanhf(0.7978845608f*(x+0.044715f*x*x*x)); h[i]=f2b(0.5f*x*(1.f+t)*b2f(u[i])); } }
extern "C" void kernel_launch(void* const* d_in, const int* in_sizes, int n_in, void* d_out, int out_size, void* d_ws, size_t ws_size, hipStream_t stream){
  const u16* x=(const u16*)d_in[0]; const u16* wqkv=(const u16*)d_in[1]; const u16* wo=(const u16*)d_in[2];
  const u16* wg=(const u16*)d_in[4]; const u16* wu=(const u16*)d_in[5]; const u16* wd=(const u16*)d_in[6];
  u16* ws=(u16*)d_ws; u16* ln=ws; u16* qkv=ln+2048*512; u16* o=qkv+2048*1536; u16* h=o+2048*512; u16* g=h+2048*512; u16* u=g+2048*1376; u16* hh=u+2048*1376;
  k_ln<<<2048,64,0,stream>>>(x,ln);
  k_gemm<<<dim3(24,2048),64,0,stream>>>(ln,wqkv,qkv,nullptr,2048,1536,512);
  k_attn<<<2048,64,0,stream>>>(qkv,o);
  k_gemm<<<dim3(8,2048),64,0,stream>>>(o,wo,h,x,2048,512,512);
  k_ln<<<2048,64,0,stream>>>(h,ln);
  k_gemm<<<dim3(22,2048),64,0,stream>>>(ln,wg,g,nullptr,2048,1376,512);
  k_gemm<<<dim3(22,2048),64,0,stream>>>(ln,wu,u,nullptr,2048,1376,512);
  k_geglu<<<(2048*1376+255)/256,256,0,stream>>>(g,u,hh,2048*1376);
  k_gemm<<<dim3(8,2048),64,0,stream>>>(hh,wd,(u16*)d_out,h,2048,512,1376);
}
```

```cpp
#include <hip/hip_runtime.h>
#include <hip/hip_bf16.h>

#if __HIP_DEVICE_COMPILE__
#pragma clang attribute push(__attribute__((target("no-packed-fp32-ops"))), apply_to = function)
#endif
#define GAS __attribute__((address_space(1)))
#define LAS __attribute__((address_space(3)))
#define DI __device__ __forceinline__
typedef unsigned u32;
typedef unsigned long long u64;
typedef u32 u32x2 __attribute__((ext_vector_type(2)));
typedef u32 u32x4 __attribute__((ext_vector_type(4)));
typedef int i32x4 __attribute__((ext_vector_type(4)));
typedef float f32x2 __attribute__((ext_vector_type(2)));
typedef float f32x4 __attribute__((ext_vector_type(4)));
typedef short s16x4 __attribute__((ext_vector_type(4)));
typedef __bf16 bf16x2 __attribute__((ext_vector_type(2)));
typedef __bf16 bf16x8 __attribute__((ext_vector_type(8)));

constexpr u32 WS_EPOCH = 0;
constexpr u32 WS_FA = 4096;
constexpr u32 WS_FB = WS_FA + 64 * 4 * 128;
constexpr u32 WS_FC = WS_FB + 64 * 128;
constexpr u32 WS_O = 131072;
constexpr u32 WS_H = WS_O + 128 * 16 * 1024;
constexpr u32 WS_HST = WS_H + 128 * 16 * 1024;
constexpr u32 WS_HH = WS_HST + 128 * 2048;
constexpr u32 WS_END = WS_HH + 128 * 43 * 1024;
constexpr u32 WS_STAMP = 16u << 20;

constexpr u32 LDS_BYTES = 163840;
constexpr u32 A_RING = 24576, A_SLOT = 34816, A_WOFF = 10240;
constexpr u32 A_SCR = 145152;
constexpr u32 A_SCR_CS = A_SCR + 16384, A_SCR_ST = A_SCR_CS + 8 * 192;
constexpr u32 L_SMALL = 61440;
constexpr u32 L_WG = 65536, L_WU = L_WG + 49152;
constexpr int NREG = 2;
constexpr u32 D_P2 = 65536;

constexpr u32 CODE_BYTES = 20480;
constexpr float LN_EPS = 1e-5f;
constexpr float LOG2E = 1.4426950408889634f;
constexpr u32 SPIN_CAP = 1u << 21;
constexpr int AUX_SC1 = 16;

#define WAITV(n) asm volatile("s_waitcnt vmcnt(" #n ")" ::: "memory")
#define BARRIER() asm volatile("s_waitcnt lgkmcnt(0)\n\ts_barrier" ::: "memory")
#define SCHED() __builtin_amdgcn_sched_barrier(0)

DI float u2f(u32 v) { return __builtin_bit_cast(float, v); }
DI u32 f2u(float v) { return __builtin_bit_cast(u32, v); }
DI i32x4 mk_rsrc(const void* p, u32 bytes) {
  const u64 a = (u64)p;
  return i32x4{(int)(u32)a, (int)((u32)(a >> 32) & 0xffffu), (int)bytes, 0x00020000};
}
DI void dma16(i32x4 rs, u32 voff, u32 soff, u32 lds) {
  asm volatile("s_mov_b32 m0, %3\n\ts_nop 0\n\tbuffer_load_dwordx4 %0, %1, %2 offen lds" ::"v"(voff), "s"(rs), "s"(soff), "s"(lds) : "memory");
}
DI void dma16m(i32x4 rs, u32 voff, u32 soff, u32 lds, u64 mask) {
  asm volatile("s_mov_b32 m0, %3\n\ts_mov_b64 exec, %4\n\tbuffer_load_dwordx4 %0, %1, %2 offen lds\n\ts_mov_b64 exec, -1" ::"v"(voff), "s"(rs), "s"(soff), "s"(lds), "s"(mask) : "memory");
}
DI u32x4 lds128(u32 a) { return *(const LAS u32x4*)a; }
DI f32x4 lds128f(u32 a) { return *(const LAS f32x4*)a; }
DI u32x4 trfrag(u32 a0, u32 a1) {
  const s16x4 lo = __builtin_amdgcn_ds_read_tr16_b64_v4i16((LAS s16x4*)a0);
  const s16x4 hi = __builtin_amdgcn_ds_read_tr16_b64_v4i16((LAS s16x4*)a1);
  const u32x2 l = __builtin_bit_cast(u32x2, lo), h = __builtin_bit_cast(u32x2, hi);
  return u32x4{l.x, l.y, h.x, h.y};
}
DI f32x4 mfma32(u32x4 a, u32x4 b, f32x4 c) {
  return __builtin_amdgcn_mfma_f32_16x16x32_bf16(__builtin_bit_cast(bf16x8, a), __builtin_bit_cast(bf16x8, b), c, 0, 0, 0);
}
DI void mma(f32x4& c, u32x4 a, u32x4 b) { asm volatile("v_mfma_f32_16x16x32_bf16 %0, %1, %2, %0" : "+v"(c) : "v"(a), "v"(b)); }
DI void mma0(f32x4& c, u32x4 a, u32x4 b) { asm volatile("v_mfma_f32_16x16x32_bf16 %0, %1, %2, 0" : "=&v"(c) : "v"(a), "v"(b)); }
#define MMA_DONE() do { asm volatile("s_nop 7\n\ts_nop 3"); __builtin_amdgcn_sched_barrier(0); } while (0)
DI u32 opaque(u32 v) { asm volatile("" : "+v"(v)); return v; }
DI f32x4 mfma16(u32x2 a, u32x2 b, f32x4 c) {
  return __builtin_amdgcn_mfma_f32_16x16x16bf16_1k(__builtin_bit_cast(s16x4, a), __builtin_bit_cast(s16x4, b), c, 0, 0, 0);
}
DI u32 pk(float a, float b) { return __builtin_bit_cast(u32, __builtin_convertvector(f32x2{a, b}, bf16x2)); }
DI float bflo(u32 v) { return u2f(v << 16); }
DI float bfhi(u32 v) { return u2f(v & 0xffff0000u); }
DI float dot2(u32 a, u32 b, float c) { return __builtin_amdgcn_fdot2_f32_bf16(__builtin_bit_cast(bf16x2, a), __builtin_bit_cast(bf16x2, b), c, false); }
template <int CTRL> DI float dpp(float v) { return u2f((u32)__builtin_amdgcn_update_dpp(0, (int)f2u(v), CTRL, 0xf, 0xf, true)); }
DI float xsum4(float v) {
  auto a = __builtin_amdgcn_permlane16_swap(f2u(v), f2u(v), false, false);
  v = u2f(a[0]) + u2f(a[1]);
  auto b = __builtin_amdgcn_permlane32_swap(f2u(v), f2u(v), false, false);
  return u2f(b[0]) + u2f(b[1]);
}
DI float xmax4(float v) {
  auto a = __builtin_amdgcn_permlane16_swap(f2u(v), f2u(v), false, false);
  v = __builtin_fmaxf(u2f(a[0]), u2f(a[1]));
  auto b = __builtin_amdgcn_permlane32_swap(f2u(v), f2u(v), false, false);
  return __builtin_fmaxf(u2f(b[0]), u2f(b[1]));
}
DI u64 mk_tag(u64 e) {
  e ^= e >> 31; e *= 0x9E3779B97F4A7C15ull; e ^= e >> 29;
  return e + 0x632BE59BD9B4E019ull;
}
DI void signal(const char* ws, u32 off, u64 tag) {
  __hip_atomic_store((GAS u64*)(ws + off), tag, __ATOMIC_RELAXED, __HIP_MEMORY_SCOPE_AGENT);
}
DI void wait_line(const char* ws, u32 off, u64 tag, int lane) {
  GAS u64* p = (GAS u64*)(ws + off + (lane & 7) * 8);
#pragma unroll 1
  for (u32 s = 0; s < SPIN_CAP; ++s) {
    const u64 v = __hip_atomic_load(p, __ATOMIC_RELAXED, __HIP_MEMORY_SCOPE_AGENT);
    if (__builtin_amdgcn_ballot_w64(v != tag) == 0) break;
    __builtin_amdgcn_s_sleep(1);
  }
  __builtin_amdgcn_fence(__ATOMIC_ACQUIRE, "wavefront");
  asm volatile("" ::: "memory");
}

#define STAMP(k) do {} while (0)
#define CSTAMP(k) do {} while (0)

__global__ __launch_bounds__(512, 2) void fused_block(const char* __restrict__ x, const char* __restrict__ wqkv, const char* __restrict__ wo,
                                                      const char* __restrict__ wg, const char* __restrict__ wu, const char* __restrict__ wd,
                                                      char* __restrict__ out, char* ws) {
#if __HIP_DEVICE_COMPILE__
  const int tid = __builtin_amdgcn_workitem_id_x(), lane = tid & 63;
  const u32 w = __builtin_amdgcn_readfirstlane(tid >> 6);
  const u32 bid = __builtin_amdgcn_workgroup_id_x(), c = bid & 7, r = bid >> 3;
  const u32 sg = w >> 2, w3 = w & 3;
  const u32 li = lane & 15, g = lane >> 4, q4 = li >> 2, p4 = lane & 3;

  const u32 hg = (c & 1) * 4 + (r & 3), pA = (c >> 1) * 8 + (r >> 2), rot = ((r >> 2) + 2 * (r & 3)) & 7;
  const i32x4 rsX = mk_rsrc(x, 2048 * 1024), rsW = mk_rsrc(wqkv, 512 * 3072);
  const u32 l8 = lane >> 3, s8 = lane & 7;
  int xrow0 = (int)(pA * 64) - 16 + (int)(8 * w + l8);
  xrow0 = xrow0 < 0 ? 0 : xrow0;
  const u32 vx0 = (u32)xrow0 * 1024 + ((s8 ^ ((4 * w + (l8 >> 1)) & 7)) << 4);
  const u32 vx1 = (pA * 64 + 48 + 2 * w + ((lane >> 3) & 1)) * 1024 + ((s8 ^ w) << 4);
  const u32 vw = (8 * w + l8) * 3072 + hg * 128 + ((s8 ^ ((((l8 >> 1) & 1) | ((w & 1) << 1)) << 1)) << 4);
  const u64 mask0 = w == 0 ? ~0xffull : ~0ull;
  auto slot_of = [](u32 i) { return A_RING + A_SLOT * ((i & 3) ^ ((i >> 1) & 1)); };
  auto piece = [&](u32 i, u32 k) {
    const u32 kc = (i + rot) & 7, sl = slot_of(i);
    if (k == 0) dma16m(rsX, vx0, kc * 128, sl + w * 1024, mask0);
    else if (k < 4) dma16(rsW, vw, kc * 64 * 3072 + (k - 1) * 1024, sl + A_WOFF + (k - 1) * 8192 + w * 1024);
    else dma16m(rsX, vx1, kc * 128, sl + 8192 + w * 256, 0xffffull);
  };
  if (w == 7) {
    u64 pc;
    asm volatile("s_getpc_b64 %0" : "=s"(pc));
    dma16m(mk_rsrc((const void*)(pc & ~127ull), CODE_BYTES), (r * 8 + (lane & 7)) * 128, 0, 0, 0xffull);
  }
#pragma unroll
  for (u32 i = 0; i < 3; ++i)
#pragma unroll
    for (u32 k = 0; k < 5; ++k) piece(i, k);
  if (w == 0 && lane < 32) *(LAS u32x4*)(A_RING + A_SLOT * (lane >> 3) + (lane & 7) * 16) = u32x4{0x3f803f80u, 0x3f803f80u, 0x3f803f80u, 0x3f803f80u};
  STAMP(12);

  const u32 pB = 4 * c + (r >> 3), cb = r & 7, qB = 2 * pB + sg;
  const u32 RC = (c >> 1) * 2 + (r >> 4), slice = (c & 1) * 16 + (r & 15), qC = 8 * RC + 2 * w3 + sg;
  const u32 sG = slice >> 3, sM = slice & 7, n3 = sG == 3 ? 4 : 6;
  const u32 U0 = 22 * sG + (sM < n3 ? 3 * sM : n3 + 2 * sM), nU = sM < n3 ? 3 : 2;
  u64 eA, eB, eC;
  asm volatile("s_load_dwordx2 %0, %3, %4\n\ts_load_dwordx2 %1, %3, %5\n\ts_load_dwordx2 %2, %3, %6\n\ts_waitcnt lgkmcnt(0)"
               : "=&s"(eA), "=&s"(eB), "=&s"(eC) : "s"(ws), "s"(pA * 8), "s"(pB * 8), "s"((4 * RC + w3) * 8) : "memory");
  const u64 tagA = mk_tag(eA), tagB = mk_tag(eB), tagC = mk_tag(eC);
  const i32x4 rsWo = mk_rsrc(wo, 512 * 1024);
  const u32 vsq = l8 * 1024 + cb * 128 + ((s8 ^ (((lane >> 4) & 3) << 1)) << 4);

  f32x4 aQ[2], aK[2], aV[2], aQ3, aK3, aV3;
  float st0 = 0.f, sq0 = 0.f, st1 = 0.f, sq1 = 0.f;
  {
    const u32 xa = li * 128 + (((g ^ (li >> 1)) & 7) << 4);
    const u32 xaF = xa ^ (sg << 6), xaO = xaF + (2 * sg + 1) * 2048, xaS = xaO ^ 64;
    const u32 wa = A_WOFF + (8 * g + q4) * 128 + (((2 * w3 + (p4 >> 1)) ^ (((q4 >> 1) | ((g & 1) << 1)) << 1)) << 4) + (p4 & 1) * 8;
    const u32 waF = wa + sg * 4096, waS = wa + (1 - sg) * 4096;
    const u32 sa0 = w * 1024 + lane * 16, sa1 = 8192 + w * 256 + li * 16;
    u32x4 F[2][13];
#pragma unroll
    for (u32 i = 0; i < 9; ++i) {
      u32x4 (&N)[13] = F[i & 1];
      u32x4 (&P)[13] = F[(i & 1) ^ 1];
      u32 x3a = 0, xoa = 0, xsa = 0, wfa = 0, wsa = 0, s0a = 0, s1a = 0;
      if (i < 8) {
        if (i < 6) WAITV(10); else if (i == 6) WAITV(9); else WAITV(8);
        BARRIER();
        if (i == 0) STAMP(13);
        if (i == 1) CSTAMP(28);
        const u32 sl = slot_of(i);
        x3a = opaque(sl + xaF); xoa = opaque(sl + xaO); xsa = opaque(sl + xaS); wfa = opaque(sl + waF); wsa = opaque(sl + waS);
        s0a = opaque(sl + sa0); s1a = opaque(sl + sa1);
      }
      auto rd = [&](u32 k) {
        if (i >= 8) return;
        switch (k) {
          case 0: N[0] = trfrag(wfa, wfa + 512); break;
          case 1: N[1] = lds128(xoa); N[2] = lds128(xoa + 2048); break;
          case 2: N[3] = lds128(x3a); N[11] = lds128(s0a); break;
          case 3: N[4] = trfrag(wfa + 8192, wfa + 8192 + 512); break;
          case 4: N[5] = trfrag(wfa + 16384, wfa + 16384 + 512); break;
          case 5: N[6] = trfrag(wsa, wsa + 512); break;
          case 6: N[7] = lds128(xsa); N[8] = lds128(xsa + 2048); break;
          case 7: N[9] = trfrag(wsa + 8192, wsa + 8192 + 512); break;
          case 8: N[10] = trfrag(wsa + 16384, wsa + 16384 + 512); break;
          case 9: N[12] = lds128(s1a); break;
        }
      };
      auto cp = [&](u32 k) {
        if (i < 5) piece(i + 3, k);
        else if (i < 7 && k < 4) { const u32 pc = ((i - 5) * 4 + k) * 8 + w; dma16(rsWo, vsq, pc * 8192, pc * 1024); }
      };
      auto mm = [&](f32x4& acc, u32 a, u32 b, bool first) { if (i == 0) return; if (first && i == 1) mma0(acc, P[a], P[b]); else mma(acc, P[a], P[b]); };
      auto dt = [&](u32 k) {
        if (i == 0) return;
        if (k < 4) { st0 = dot2(P[11][k], 0x3f803f80u, st0); sq0 = dot2(P[11][k], P[11][k], sq0); }
        else { st1 = dot2(P[12][k - 4], 0x3f803f80u, st1); sq1 = dot2(P[12][k - 4], P[12][k - 4], sq1); }
      };
      SCHED();
      mm(aQ[0], 0, 1, true); rd(0); SCHED();
      mm(aQ[1], 0, 2, true); rd(1); cp(0); SCHED();
      mm(aQ3, 0, 3, true); rd(2); SCHED();
      mm(aK[0], 4, 1, true); rd(3); SCHED();
      mm(aK[1], 4, 2, true); rd(4); cp(1); SCHED();
      mm(aK3, 4, 3, true); rd(5); SCHED();
      mm(aV[0], 1, 5, true); rd(6); SCHED();
      mm(aV[1], 2, 5, true); rd(7); cp(2); SCHED();
      mm(aV3, 3, 5, true); rd(8); dt(0); SCHED();
      mm(aQ[0], 6, 7, false); rd(9); dt(1); SCHED();
      mm(aQ[1], 6, 8, false); cp(3); dt(2); SCHED();
      mm(aK[0], 9, 7, false); dt(3); dt(4); SCHED();
      mm(aK[1], 9, 8, false); dt(5); SCHED();
      mm(aV[0], 7, 10, false); cp(4); dt(6); SCHED();
      mm(aV[1], 8, 10, false); dt(7); SCHED();
    }
    MMA_DONE();
  }
  CSTAMP(29);
  STAMP(1);
  const __amdgpu_buffer_rsrc_t rWS = __builtin_amdgcn_make_buffer_rsrc(ws, 0, WS_END, 0x00020000);
  {
    st0 += dpp<0xB1>(st0); sq0 += dpp<0xB1>(sq0); st1 += dpp<0xB1>(st1); sq1 += dpp<0xB1>(sq1);
    st0 += dpp<0x4E>(st0); sq0 += dpp<0x4E>(sq0); st1 += dpp<0x4E>(st1); sq1 += dpp<0x4E>(sq1);
    st0 += dpp<0x141>(st0); sq0 += dpp<0x141>(sq0); st1 += dpp<0x141>(st1); sq1 += dpp<0x141>(sq1);
    const float m0 = st0 * (1.f / 512), m1 = st1 * (1.f / 512);
    const float r0 = __builtin_amdgcn_rsqf(__builtin_fmaxf(sq0 * (1.f / 512) - m0 * m0, 0.f) + LN_EPS);
    const float r1 = __builtin_amdgcn_rsqf(__builtin_fmaxf(sq1 * (1.f / 512) - m1 * m1, 0.f) + LN_EPS);
    if (s8 == 0) {
      *(LAS f32x2*)(A_SCR_ST + (8 * w + l8) * 8) = f32x2{m0, r0};
      if (lane < 16) *(LAS f32x2*)(A_SCR_ST + (64 + 2 * w + l8) * 8) = f32x2{m1, r1};
    }
    *(LAS f32x4*)(A_SCR + w * 2048 + lane * 16) = sg ? aK3 : aK[1];
    *(LAS f32x4*)(A_SCR + w * 2048 + 1024 + lane * 16) = sg ? aV3 : aV[1];
    if (li == 0) { *(LAS f32x4*)(A_SCR_CS + w * 192 + g * 16) = aQ3; *(LAS f32x4*)(A_SCR_CS + w * 192 + 64 + g * 16) = aK3; }
    if (lane < 16) *(LAS float*)(A_SCR_CS + w * 192 + 128 + lane * 4) = aV3[0];
    BARRIER();
    const u32 pw = w ^ 4;
    f32x4 pK = lds128f(A_SCR + pw * 2048 + lane * 16), pV = lds128f(A_SCR + pw * 2048 + 1024 + lane * 16);
    if (sg == 0) { pK += aK3; pV += aV3; }
    const f32x4 csq = lds128f(A_SCR_CS + w * 192 + g * 16) + lds128f(A_SCR_CS + pw * 192 + g * 16);
    const f32x4 csk = lds128f(A_SCR_CS + w * 192 + 64 + g * 16) + lds128f(A_SCR_CS + pw * 192 + 64 + g * 16);
    const float csv = *(const LAS float*)(A_SCR_CS + w * 192 + 128 + li * 4) + *(const LAS float*)(A_SCR_CS + pw * 192 + 128 + li * 4);
    u32x2 Kp[3], Vp[3], Qp[2];
    float rq[2];
#pragma unroll
    for (int b = 0; b < 3; ++b) {
      const u32 sb = A_SCR_ST + (2 * sg + b) * 128;
      const f32x2 s = *(const LAS f32x2*)(sb + li * 8);
      const f32x4 t0 = lds128f(sb + g * 32), t1 = lds128f(sb + g * 32 + 16);
      const f32x4 k = b == 0 ? pK : aK[b - 1], v = b == 0 ? pV : aV[b - 1];
      Kp[b] = u32x2{pk((k[0] - s.x * csk[0]) * s.y, (k[1] - s.x * csk[1]) * s.y), pk((k[2] - s.x * csk[2]) * s.y, (k[3] - s.x * csk[3]) * s.y)};
      Vp[b] = u32x2{pk((v[0] - t0[0] * csv) * t0[1], (v[1] - t0[2] * csv) * t0[3]), pk((v[2] - t1[0] * csv) * t1[1], (v[3] - t1[2] * csv) * t1[3])};
      if (b > 0) {
        const f32x4 q = aQ[b - 1];
        Qp[b - 1] = u32x2{pk(q[0] - s.x * csq[0], q[1] - s.x * csq[1]), pk(q[2] - s.x * csq[2], q[3] - s.x * csq[3])};
        rq[b - 1] = s.y * (0.25f * LOG2E);
      }
    }
    const bool par = li & 1;
    const u32 kA = 4 * g + par;
    const bool curA = kA <= li, curB = kA + 2 <= li;
    const u32 selA = par ? 0x01000c0cu : 0x0c0c0100u, selB = par ? 0x03020c0cu : 0x0c0c0302u, selZ = 0x0c0c0c0cu;
    const bool first = pA == 0 && sg == 0;
    u32x2 Op[2];
#pragma unroll
    for (int b = 0; b < 2; ++b) {
      const f32x4 z = {0.f, 0.f, 0.f, 0.f};
      const f32x4 sc = mfma16(Kp[b + 1], Qp[b], z), sp = mfma16(Kp[b], Qp[b], z);
      float pa = par ? sp[1] : sp[0], pb = par ? sp[3] : sp[2];
      if (b == 0 && first) { pa = -1e30f; pb = -1e30f; }
      const float la = curA ? (par ? sc[1] : sc[0]) : pa, lb = curB ? (par ? sc[3] : sc[2]) : pb;
      const float m = xmax4(__builtin_fmaxf(la, lb));
      const float ea = __builtin_amdgcn_exp2f((la - m) * rq[b]), eb = __builtin_amdgcn_exp2f((lb - m) * rq[b]);
      const float inv = __builtin_amdgcn_rcpf(xsum4(ea + eb));
      const u32 pp = pk(ea * inv, eb * inv);
      const u32x4 P = {__builtin_amdgcn_perm(pp, pp, curA ? selZ : selA), __builtin_amdgcn_perm(pp, pp, curB ? selZ : selB),
                       __builtin_amdgcn_perm(pp, pp, curA ? selA : selZ), __builtin_amdgcn_perm(pp, pp, curB ? selB : selZ)};
      const f32x4 o = mfma32(u32x4{Vp[b].x, Vp[b].y, Vp[b + 1].x, Vp[b + 1].y}, P, z);
      Op[b] = u32x2{pk(o[0], o[1]), pk(o[2], o[3])};
    }
    const auto s0 = __builtin_amdgcn_permlane32_swap(Op[0].x, Op[1].x, false, false);
    const auto s1 = __builtin_amdgcn_permlane32_swap(Op[0].y, Op[1].y, false, false);
    const u32 H = 4 * hg + w3;
    __builtin_amdgcn_raw_buffer_store_b128(u32x4{s0[0], s1[0], s0[1], s1[1]}, rWS,
        WS_O + ((4 * pA + 2 * sg + (lane >> 5)) * 16 + (H >> 1)) * 1024 + (H & 1) * 512 + (lane & 31) * 16, 0, AUX_SC1);
  }
  WAITV(0);
  if (lane == 0) signal(ws, WS_FA + ((2 * pA + sg) * 4 + (hg >> 1)) * 128 + ((hg & 1) * 4 + w3) * 8, tagA);
  STAMP(2);
  const i32x4 rsG = mk_rsrc(wg, 512 * 2752), rsU = mk_rsrc(wu, 512 * 2752);
  u32 vgu[3];
#pragma unroll
  for (u32 t = 0; t < 3; ++t) {
    const u32 o = 64 * t + lane, row = o / 6, ch = o - row * 6;
    vgu[t] = ch < 2 * nU ? row * 2752 + U0 * 32 + ch * 16 : 0x80000000u;
  }
#pragma unroll
  for (u32 k = 0; k < 2; ++k)
#pragma unroll
    for (u32 t = 0; t < 3; ++t) dma16(rsG, vgu[t], (2 * w + k) * 32 * 2752, L_WG + ((2 * w + k) * 3 + t) * 1024);
#pragma unroll
  for (u32 t = 0; t < 3; ++t) dma16(rsU, vgu[t], w * 32 * 2752, L_WU + (w * 3 + t) * 1024);

  const u32 bF = w3 >> 1, sF = w3 & 1;
  const __amdgpu_buffer_rsrc_t rX = __builtin_amdgcn_make_buffer_rsrc((void*)x, 0, 2048 * 1024, 0x00020000);
  const u32 xoff = (32 * qB + 16 * bF + li) * 1024 + cb * 128 + sF * 64 + g * 8;
  const u32x2 xr0 = __builtin_amdgcn_raw_buffer_load_b64(rX, xoff, 0, 0), xr1 = __builtin_amdgcn_raw_buffer_load_b64(rX, xoff + 32, 0, 0);
  BARRIER();
  u32 sqa[4];
  {
    const u32 a = (16 * (g >> 1) + 4 * (g & 1) + q4) * 128 + ((((2 * (g & 1) + (q4 >> 1)) << 1) + (p4 >> 1)) << 4) + (p4 & 1) * 8;
#pragma unroll
    for (u32 u = 0; u < 4; ++u) sqa[u] = a ^ ((u ^ (2 * sF)) << 5);
  }
  u32x2 hp[2];
  const u32 red = sg * 24576;
  {
    u32x4 fw[4][4];
#pragma unroll
    for (u32 s = 0; s < 4; ++s)
#pragma unroll
      for (u32 u = 0; u < 4; ++u) { const u32 a = sqa[u] + w3 * 16384 + s * 4096; fw[s][u] = trfrag(a, a + 1024); }
    BARRIER();
    wait_line(ws, WS_FA + (qB * 4 + w3) * 128, tagB, lane);
    STAMP(3);
    u32x4 fa[2][4];
#pragma unroll
    for (u32 s = 0; s < 4; ++s)
#pragma unroll
      for (u32 b = 0; b < 2; ++b)
        fa[b][s] = __builtin_amdgcn_raw_buffer_load_b128(rWS, WS_O + lane * 16, ((2 * qB + (b ^ bF)) * 16 + 4 * w3 + s) * 1024, AUX_SC1);
    SCHED();
    f32x4 acc[2][4];
#pragma unroll
    for (u32 s = 0; s < 4; ++s)
#pragma unroll
      for (u32 b = 0; b < 2; ++b)
#pragma unroll
        for (u32 u = 0; u < 4; ++u) { if (s == 0) mma0(acc[b][u], fw[s][u], fa[b][s]); else mma(acc[b][u], fw[s][u], fa[b][s]); }
    MMA_DONE();
#pragma unroll
    for (u32 d = 1; d < 4; ++d) {
      const u32 a = red + ((w3 ^ d) * 3 + d - 1) * 2048 + lane * 16;
      *(LAS f32x4*)a = acc[d >> 1][(d & 1) * 2];
      *(LAS f32x4*)(a + 1024) = acc[d >> 1][(d & 1) * 2 + 1];
    }
    BARRIER();
    f32x4 h0 = acc[0][0], h1 = acc[0][1];
#pragma unroll
    for (u32 d = 0; d < 3; ++d) { const u32 a = red + (w3 * 3 + d) * 2048 + lane * 16; h0 += lds128f(a); h1 += lds128f(a + 1024); }
    hp[0] = u32x2{pk(h0[0] + bflo(xr0.x), h0[1] + bfhi(xr0.x)), pk(h0[2] + bflo(xr0.y), h0[3] + bfhi(xr0.y))};
    hp[1] = u32x2{pk(h1[0] + bflo(xr1.x), h1[1] + bfhi(xr1.x)), pk(h1[2] + bflo(xr1.y), h1[3] + bfhi(xr1.y))};
    __builtin_amdgcn_raw_buffer_store_b128(u32x4{hp[0].x, hp[0].y, hp[1].x, hp[1].y}, rWS, WS_H + lane * 16 + ((2 * qB + bF) * 16 + 2 * cb + sF) * 1024, 0, AUX_SC1);
    float s = dot2(hp[0].x, 0x3f803f80u, 0.f), qq = dot2(hp[0].x, hp[0].x, 0.f);
    s = dot2(hp[0].y, 0x3f803f80u, s); qq = dot2(hp[0].y, hp[0].y, qq);
    s = dot2(hp[1].x, 0x3f803f80u, s); qq = dot2(hp[1].x, hp[1].x, qq);
    s = dot2(hp[1].y, 0x3f803f80u, s); qq = dot2(hp[1].y, hp[1].y, qq);
    s = xsum4(s); qq = xsum4(qq);
    if (lane < 16)
      __builtin_amdgcn_raw_buffer_store_b64(u32x2{f2u(s), f2u(qq)}, rWS, WS_HST + lane * 8, ((2 * qB + bF) * 16 + 2 * cb + sF) * 128, AUX_SC1);
    WAITV(0);
    BARRIER();
    if (w3 == 0 && lane == 0) signal(ws, WS_FB + qB * 128 + cb * 8, tagB);
  }
  STAMP(4);
#pragma unroll
  for (u32 t = 0; t < 3; ++t) dma16(rsU, vgu[t], (8 + w) * 32 * 2752, L_WU + ((8 + w) * 3 + t) * 1024);

  const i32x4 rsD = mk_rsrc(wd, 1376 * 1024);
  u32x4 fd[NREG][4];
  {
#pragma unroll
    for (u32 k = 0; k < 2 * NREG; ++k) {
      const u32 i = k >> 1, pc = sg * 2 + (k & 1);
      dma16(rsD, vsq, ((11 * w3 + i) * 4 + pc) * 8192, ((4 * i + w3) * 4 + pc) * 1024);
    }
    WAITV(0);
    BARRIER();
#pragma unroll
    for (u32 i = 0; i < NREG; ++i)
#pragma unroll
      for (u32 u = 0; u < 4; ++u) { const u32 a = sqa[u] + (4 * i + w3) * 4096; fd[i][u] = trfrag(a, a + 1024); }
    const u32 ca = (4 * g + q4) * 96 + p4 * 8;
    if (w < 6) {
      const u32 a = opaque((w < 3 ? L_WG + w * 32 : L_WU + (w - 3) * 32) + ca);
      const u32x4 ones = {0x3f803f80u, 0x3f803f80u, 0x3f803f80u, 0x3f803f80u};
      f32x4 cs = {};
#pragma unroll
      for (u32 s = 0; s < 16; ++s) cs = mfma32(trfrag(a + s * 3072, a + s * 3072 + 1536), ones, cs);
      if (li == 0) *(LAS f32x4*)(L_SMALL + w * 64 + g * 16) = cs;
    }
    BARRIER();
  }
  STAMP(5);

  {
    wait_line(ws, WS_FB + qC * 128, tagC, lane);
    STAMP(6); CSTAMP(30);
    u32x2 sp[2][4];
#pragma unroll
    for (u32 b = 0; b < 2; ++b)
#pragma unroll
      for (u32 k = 0; k < 4; ++k) sp[b][k] = __builtin_amdgcn_raw_buffer_load_b64(rWS, WS_HST + (4 * g + k) * 128 + li * 8, (2 * qC + b) * 2048, AUX_SC1);
    constexpr int WIN = 8;
    u32x4 fa[16][2];
    auto ldA = [&](u32 s) {
#pragma unroll
      for (u32 b = 0; b < 2; ++b) fa[s][b] = __builtin_amdgcn_raw_buffer_load_b128(rWS, WS_H + lane * 16, ((2 * qC + b) * 16 + s) * 1024, AUX_SC1);
    };
#pragma unroll
    for (u32 s = 0; s < WIN; ++s) ldA(s);
    SCHED();
    const u32 aG = opaque(L_WG + (4 * g + q4) * 96 + p4 * 8), aU = opaque(L_WU + (4 * g + q4) * 96 + p4 * 8);
    f32x4 cg[2][3], cu[2][3];
    u32x4 fg[3], fu[3];
#pragma unroll
    for (u32 u = 0; u < 3; ++u) { fg[u] = trfrag(aG + u * 32, aG + 1536 + u * 32); fu[u] = trfrag(aU + u * 32, aU + 1536 + u * 32); }
    SCHED();
#pragma unroll
    for (u32 s = 0; s < 16; ++s) {
#pragma unroll
      for (u32 u = 0; u < 3; ++u) {
        const u32 o = (s + 1) * 3072 + u * 32;
        if (s == 0) { mma0(cg[0][u], fg[u], fa[s][0]); mma0(cg[1][u], fg[u], fa[s][1]); } else { mma(cg[0][u], fg[u], fa[s][0]); mma(cg[1][u], fg[u], fa[s][1]); }
        SCHED();
        if (s < 15) fg[u] = trfrag(aG + o, aG + o + 1536);
        SCHED();
        if (s == 0) { mma0(cu[0][u], fu[u], fa[s][0]); mma0(cu[1][u], fu[u], fa[s][1]); } else { mma(cu[0][u], fu[u], fa[s][0]); mma(cu[1][u], fu[u], fa[s][1]); }
        SCHED();
        if (s < 15) fu[u] = trfrag(aU + o, aU + o + 1536);
        if (u < 2 && s + WIN < 16) fa[s + WIN][u] = __builtin_amdgcn_raw_buffer_load_b128(rWS, WS_H + lane * 16, ((2 * qC + u) * 16 + s + WIN) * 1024, AUX_SC1);
        if (u == 1 && s >= 8 && s < 14) {
          const u32 k = s - 8, i = k >> 1, pc = sg * 2 + (k & 1);
          dma16(rsD, vsq, ((11 * w3 + NREG + i) * 4 + pc) * 8192, ((4 * i + w3) * 4 + pc) * 1024);
        }
        SCHED();
      }
    }
    MMA_DONE();
    STAMP(7); CSTAMP(31);
    float mean[2], rstd[2];
#pragma unroll
    for (u32 b = 0; b < 2; ++b) {
      float s = (u2f(sp[b][0].x) + u2f(sp[b][1].x)) + (u2f(sp[b][2].x) + u2f(sp[b][3].x));
      float qq = (u2f(sp[b][0].y) + u2f(sp[b][1].y)) + (u2f(sp[b][2].y) + u2f(sp[b][3].y));
      s = xsum4(s); qq = xsum4(qq);
      mean[b] = s * (1.f / 512);
      rstd[b] = __builtin_amdgcn_rsqf(__builtin_fmaxf(qq * (1.f / 512) - mean[b] * mean[b], 0.f) + LN_EPS);
    }
    constexpr float C1 = -2.f * 0.7978845608028654f * LOG2E, C2 = C1 * 0.044715f;
#pragma unroll
    for (u32 u = 0; u < 3; ++u) {
      const f32x4 sg_ = lds128f(L_SMALL + u * 64 + g * 16), su_ = lds128f(L_SMALL + (3 + u) * 64 + g * 16);
      u32x2 o[2];
#pragma unroll
      for (u32 b = 0; b < 2; ++b) {
        float v[4];
#pragma unroll
        for (u32 k = 0; k < 4; ++k) {
          const float gg = (cg[b][u][k] - mean[b] * sg_[k]) * rstd[b], uu = (cu[b][u][k] - mean[b] * su_[k]) * rstd[b];
          const float e = __builtin_amdgcn_exp2f(gg * (C1 + C2 * gg * gg));
          v[k] = gg * uu * __builtin_amdgcn_rcpf(1.f + e);
        }
        o[b] = u32x2{pk(v[0], v[1]), pk(v[2], v[3])};
      }
      const auto s0 = __builtin_amdgcn_permlane32_swap(o[0].x, o[1].x, false, false);
      const auto s1 = __builtin_amdgcn_permlane32_swap(o[0].y, o[1].y, false, false);
      const u32 U = U0 + u;
      if (u < nU)
        __builtin_amdgcn_raw_buffer_store_b128(u32x4{s0[0], s1[0], s0[1], s1[1]}, rWS,
            WS_HH + (lane >> 5) * 43 * 1024 + (lane & 31) * 16 + (2 * qC * 43 + (U >> 1)) * 1024 + (U & 1) * 512, 0, AUX_SC1);
    }
    WAITV(0);
    if (lane == 0) signal(ws, WS_FC + (qC * 4 + sG) * 128 + sM * 8, tagC);
    STAMP(8);
    BARRIER();
  }

  {
    constexpr u32 NP2 = 11 - NREG - 3;
#pragma unroll
    for (u32 k = 0; k < 2 * NP2; ++k) {
      const u32 i = k >> 1, pc = sg * 2 + (k & 1);
      dma16(rsD, vsq, ((11 * w3 + NREG + 3 + i) * 4 + pc) * 8192, D_P2 + ((4 * i + w3) * 4 + pc) * 1024);
    }
    wait_line(ws, WS_FC + (qB * 4 + w3) * 128, tagB, lane);
    STAMP(9);
    u32x4 fa[11][2];
#pragma unroll
    for (u32 i = 0; i < 11; ++i)
#pragma unroll
      for (u32 b = 0; b < 2; ++b) {
        const u32 ks = (i == 10 && w3 == 3) ? 42 : 11 * w3 + i;
        fa[i][b] = __builtin_amdgcn_raw_buffer_load_b128(rWS, WS_HH + lane * 16, ((2 * qB + (b ^ bF)) * 43 + ks) * 1024, AUX_SC1);
      }
    BARRIER();
    f32x4 acc[2][4];
    u32 da[4], db[4];
#pragma unroll
    for (u32 u = 0; u < 4; ++u) { da[u] = opaque(sqa[u] + w3 * 4096); db[u] = opaque(sqa[u] + w3 * 4096 + D_P2); }
    auto ldW = [&](u32 i, u32 u) {
      const u32 j = i - NREG;
      const u32 a = j < 3 ? da[u] + j * 16384 : (j < 6 ? db[u] + (j - 3) * 16384 : db[u] + 49152 + (j - 6) * 16384);
      return trfrag(a, a + 1024);
    };
    u32x4 fl[4];
#pragma unroll
    for (u32 u = 0; u < 4; ++u) fl[u] = ldW(NREG, u);
    SCHED();
#pragma unroll
    for (u32 i = 0; i < 11; ++i) {
#pragma unroll
      for (u32 u = 0; u < 4; ++u) {
        const u32x4 f = i < NREG ? fd[i][u] : fl[u];
        if (i == 0) { mma0(acc[0][u], f, fa[i][0]); mma0(acc[1][u], f, fa[i][1]); } else { mma(acc[0][u], f, fa[i][0]); mma(acc[1][u], f, fa[i][1]); }
        SCHED();
        if (i >= NREG && i < 10) fl[u] = ldW(i + 1, u);
        SCHED();
      }
    }
    MMA_DONE();
    STAMP(10);
    BARRIER();
#pragma unroll
    for (u32 d = 1; d < 4; ++d) {
      const u32 a = red + ((w3 ^ d) * 3 + d - 1) * 2048 + lane * 16;
      *(LAS f32x4*)a = acc[d >> 1][(d & 1) * 2];
      *(LAS f32x4*)(a + 1024) = acc[d >> 1][(d & 1) * 2 + 1];
    }
    BARRIER();
    f32x4 h0 = acc[0][0], h1 = acc[0][1];
#pragma unroll
    for (u32 d = 0; d < 3; ++d) { const u32 a = red + (w3 * 3 + d) * 2048 + lane * 16; h0 += lds128f(a); h1 += lds128f(a + 1024); }
    const u32 x0 = pk(h0[0] + bflo(hp[0].x), h0[1] + bfhi(hp[0].x)), x1 = pk(h0[2] + bflo(hp[0].y), h0[3] + bfhi(hp[0].y));
    const u32 y0 = pk(h1[0] + bflo(hp[1].x), h1[1] + bfhi(hp[1].x)), y1 = pk(h1[2] + bflo(hp[1].y), h1[3] + bfhi(hp[1].y));
    const auto s0 = __builtin_amdgcn_permlane16_swap(x0, y0, false, false);
    const auto s1 = __builtin_amdgcn_permlane16_swap(x1, y1, false, false);
    *(GAS u32x4*)(out + (size_t)((32 * qB + 16 * bF + li) * 1024 + cb * 128 + sF * 64 + (g & 1) * 32 + (g >> 1) * 16)) = u32x4{s0[0], s1[0], s0[1], s1[1]};
    if (cb == 0 && tid == 0) signal(ws, WS_EPOCH + pB * 8, eB + 1);
    STAMP(11);
  }
#endif
}

#if __HIP_DEVICE_COMPILE__
#pragma clang attribute pop
#endif

extern "C" void kernel_launch(void* const* d_in, const int* in_sizes, int n_in, void* d_out, int out_size, void* d_ws, size_t ws_size,
                              hipStream_t stream) {
  (void)hipFuncSetAttribute((const void*)fused_block, hipFuncAttributeMaxDynamicSharedMemorySize, LDS_BYTES);
  fused_block<<<256, 512, LDS_BYTES, stream>>>((const char*)d_in[0], (const char*)d_in[1], (const char*)d_in[2], (const char*)d_in[4],
                                               (const char*)d_in[5], (const char*)d_in[6], (char*)d_out, (char*)d_ws);
}
```

```cpp
#include <hip/hip_runtime.h>
#include <hip/hip_bf16.h>

#if __HIP_DEVICE_COMPILE__
#pragma clang attribute push(__attribute__((target("no-packed-fp32-ops"))), apply_to = function)
#endif
#define GAS __attribute__((address_space(1)))
#define LAS __attribute__((address_space(3)))
#define DI __device__ __forceinline__
typedef unsigned u32;
typedef unsigned long long u64;
typedef u32 u32x2 __attribute__((ext_vector_type(2)));
typedef u32 u32x4 __attribute__((ext_vector_type(4)));
typedef int i32x4 __attribute__((ext_vector_type(4)));
typedef float f32x2 __attribute__((ext_vector_type(2)));
typedef float f32x4 __attribute__((ext_vector_type(4)));
typedef short s16x4 __attribute__((ext_vector_type(4)));
typedef __bf16 bf16x2 __attribute__((ext_vector_type(2)));
typedef __bf16 bf16x8 __attribute__((ext_vector_type(8)));

constexpr u32 WS_EPOCH = 0;
constexpr u32 WS_FA = 4096;
constexpr u32 WS_FB = WS_FA + 64 * 4 * 128;
constexpr u32 WS_FC = WS_FB + 64 * 128;
constexpr u32 WS_ST = 81920;
constexpr u32 WS_O = 131072;
constexpr u32 WS_H = WS_O + 128 * 16 * 1024;
constexpr u32 WS_HST = WS_H + 128 * 16 * 1024;
constexpr u32 WS_HH = WS_HST + 128 * 2048;
constexpr u32 WS_END = WS_HH + 128 * 43 * 1024;
constexpr u32 WS_STAMP = 16u << 20;

constexpr u32 LDS_BYTES = 163840;
constexpr u32 A_RING = 23552, A_SLOT = 34816, A_WOFF = 10240;
constexpr u32 A_SCR = 144256;
constexpr u32 A_SCR_CS = A_SCR + 16384, A_SCR_ST = A_SCR_CS + 8 * 192;
constexpr u32 L_WG = 64512, L_WU = L_WG + 49152;
constexpr u32 L_CTL = 162816;
constexpr u32 L_CS = L_CTL + 256, L_SINK = L_CTL + 768;
constexpr u32 D_SCR = 16384;
enum { C_AS, C_WL, C_WO, C_WGU, C_CS, C_BS, C_P1, C_CD, C_PULL, C_P2, C_PB, C_D0, C_B2  , C_B3 = C_B2 + 4  , C_D2 = C_B3 + 2  , C_N = C_D2 + 2 };

constexpr u32 CODE_BYTES = 26880;
struct On { static constexpr bool value = true; };
struct Off { static constexpr bool value = false; };
#define EARLY 12
#define PACE() do {} while (0)
#define ROT (2 * ((r >> 2) + (r & 3)))
#define TOUCH_AT 7
constexpr float LN_EPS = 1e-5f;
constexpr float LOG2E = 1.4426950408889634f;
constexpr u32 SPIN_CAP = 1u << 21;
constexpr int AUX_SC1 = 16;

#define WAITV(n) asm volatile("s_waitcnt vmcnt(" #n ")" ::: "memory")
#define BARRIER() do { __builtin_amdgcn_s_waitcnt(0xC07F); asm volatile("s_barrier" ::: "memory"); } while (0)
#define SCHED() __builtin_amdgcn_sched_barrier(0)

DI float u2f(u32 v) { return __builtin_bit_cast(float, v); }
DI u32 f2u(float v) { return __builtin_bit_cast(u32, v); }
DI i32x4 mk_rsrc(const void* p, u32 bytes) {
  const u64 a = (u64)p;
  return i32x4{(int)(u32)a, (int)((u32)(a >> 32) & 0xffffu), (int)bytes, 0x00020000};
}
DI void dma16(i32x4 rs, u32 voff, u32 soff, u32 lds) {
  asm volatile("s_mov_b32 m0, %3\n\ts_nop 0\n\tbuffer_load_dwordx4 %0, %1, %2 offen lds" ::"v"(voff), "s"(rs), "s"(soff), "s"(lds) : "memory");
}
DI void dma16m(i32x4 rs, u32 voff, u32 soff, u32 lds, u64 mask) {
  asm volatile("s_mov_b32 m0, %3\n\ts_mov_b64 exec, %4\n\tbuffer_load_dwordx4 %0, %1, %2 offen lds\n\ts_mov_b64 exec, -1" ::"v"(voff), "s"(rs), "s"(soff), "s"(lds), "s"(mask) : "memory");
}
DI void dma4(i32x4 rs, u32 voff, u32 lds) {
  asm volatile("s_mov_b32 m0, %2\n\ts_nop 0\n\tbuffer_load_dword %0, %1, 0 offen lds" ::"v"(voff), "s"(rs), "s"(lds) : "memory");
}
DI void ld_sc1(u32x4& d, i32x4 rs, u32 voff) {
  asm volatile("buffer_load_dwordx4 %0, %1, %2, 0 offen sc1" : "=&v"(d) : "v"(voff), "s"(rs) : "memory");
}
DI u32x4 lds128(u32 a) { return *(const LAS u32x4*)a; }
DI f32x4 lds128f(u32 a) { return *(const LAS f32x4*)a; }
DI u32x4 trfrag(u32 a0, u32 a1) {
  const s16x4 lo = __builtin_amdgcn_ds_read_tr16_b64_v4i16((LAS s16x4*)a0);
  const s16x4 hi = __builtin_amdgcn_ds_read_tr16_b64_v4i16((LAS s16x4*)a1);
  const u32x2 l = __builtin_bit_cast(u32x2, lo), h = __builtin_bit_cast(u32x2, hi);
  return u32x4{l.x, l.y, h.x, h.y};
}
DI f32x4 mfma32(u32x4 a, u32x4 b, f32x4 c) {
  return __builtin_amdgcn_mfma_f32_16x16x32_bf16(__builtin_bit_cast(bf16x8, a), __builtin_bit_cast(bf16x8, b), c, 0, 0, 0);
}
DI void mma(f32x4& c, u32x4 a, u32x4 b) { asm volatile("v_mfma_f32_16x16x32_bf16 %0, %1, %2, %0" : "+v"(c) : "v"(a), "v"(b)); }
DI void mma0(f32x4& c, u32x4 a, u32x4 b) { asm volatile("v_mfma_f32_16x16x32_bf16 %0, %1, %2, 0" : "=&v"(c) : "v"(a), "v"(b)); }
#define MMA_DONE() do { asm volatile("s_nop 7\n\ts_nop 3"); __builtin_amdgcn_sched_barrier(0); } while (0)
DI u32 opaque(u32 v) { asm volatile("" : "+v"(v)); return v; }
DI f32x4 mfma16(u32x2 a, u32x2 b, f32x4 c) {
  return __builtin_amdgcn_mfma_f32_16x16x16bf16_1k(__builtin_bit_cast(s16x4, a), __builtin_bit_cast(s16x4, b), c, 0, 0, 0);
}
DI u32 pk(float a, float b) { return __builtin_bit_cast(u32, __builtin_convertvector(f32x2{a, b}, bf16x2)); }
DI float bflo(u32 v) { return u2f(v << 16); }
DI float bfhi(u32 v) { return u2f(v & 0xffff0000u); }
DI float dot2(u32 a, u32 b, float c) { return __builtin_amdgcn_fdot2_f32_bf16(__builtin_bit_cast(bf16x2, a), __builtin_bit_cast(bf16x2, b), c, false); }
DI void dot2v(float& c, u32 a, u32 b) { asm volatile("v_dot2c_f32_bf16 %0, %1, %2" : "+v"(c) : "s"(a), "v"(b)); }
DI void dot2q(float& c, u32 a) { asm volatile("v_dot2c_f32_bf16 %0, %1, %1" : "+v"(c) : "v"(a)); }
template <int CTRL> DI float dpp(float v) { return u2f((u32)__builtin_amdgcn_update_dpp(0, (int)f2u(v), CTRL, 0xf, 0xf, true)); }
DI float xsum4(float v) {
  auto a = __builtin_amdgcn_permlane16_swap(f2u(v), f2u(v), false, false);
  v = u2f(a[0]) + u2f(a[1]);
  auto b = __builtin_amdgcn_permlane32_swap(f2u(v), f2u(v), false, false);
  return u2f(b[0]) + u2f(b[1]);
}
DI float fmx(float a, float b) { float r; asm("v_max_f32 %0, %1, %2" : "=v"(r) : "v"(a), "v"(b)); return r; }
DI float opq(float v) { asm("" : "+v"(v)); return v; }
DI float sel(bool c, float a, float b) { a = opq(a); b = opq(b); return c ? a : b; }
DI float xmax4(float v) {
  auto a = __builtin_amdgcn_permlane16_swap(f2u(v), f2u(v), false, false);
  v = fmx(u2f(a[0]), u2f(a[1]));
  auto b = __builtin_amdgcn_permlane32_swap(f2u(v), f2u(v), false, false);
  return fmx(u2f(b[0]), u2f(b[1]));
}
DI u64 mk_tag(u64 e) {
  e ^= e >> 31; e *= 0x9E3779B97F4A7C15ull; e ^= e >> 29;
  return e + 0x632BE59BD9B4E019ull;
}
DI u64 epoch(u32x2 v, int l) { return (u64)(u32)__builtin_amdgcn_readlane(v.x, l) | ((u64)(u32)__builtin_amdgcn_readlane(v.y, l) << 32); }
DI void signal(const char* ws, u32 off, u64 tag) {
  __hip_atomic_store((GAS u64*)(ws + off), tag, __ATOMIC_RELAXED, __HIP_MEMORY_SCOPE_AGENT);
}
DI void wait_tags(const char* ws, u32 off, u64 tag) {
  GAS u64* p = (GAS u64*)(ws + off);
#pragma unroll 1
  for (u32 s = 0; s < SPIN_CAP; ++s) {
    const u64 v = __hip_atomic_load(p, __ATOMIC_RELAXED, __HIP_MEMORY_SCOPE_AGENT);
    if (__builtin_amdgcn_ballot_w64(v != tag) == 0) break;
    __builtin_amdgcn_s_sleep(1);
  }
  __builtin_amdgcn_fence(__ATOMIC_ACQUIRE, "wavefront");
  asm volatile("" ::: "memory");
}
DI void arrive(u32 c) {
  asm volatile("s_mov_b64 exec, 1\n\tds_add_u32 %0, %1\n\ts_mov_b64 exec, -1" ::"v"(L_CTL + 4 * c), "v"(1u) : "memory");
}
DI u32 arrive_old(u32 c) {
  u32 o;
  asm volatile("s_mov_b64 exec, 1\n\tds_add_rtn_u32 %0, %1, %2\n\ts_mov_b64 exec, -1\n\ts_waitcnt lgkmcnt(0)" : "=&v"(o) : "v"(L_CTL + 4 * c), "v"(1u) : "memory");
  return __builtin_amdgcn_readfirstlane(o);
}
DI void await(u32 c, u32 n) {
#pragma unroll 1
  for (u32 s = 0; s < SPIN_CAP * 4; ++s) {
    const u32 v = *(volatile LAS u32*)(L_CTL + 4 * c);
    if ((u32)__builtin_amdgcn_readfirstlane(v) >= n) break;
  }
  asm volatile("" ::: "memory");
}
DI u64 opaque_s(u64 v) { asm volatile("" : "+s"(v)); return v; }

#define STAMP(k) do {} while (0)
#define STAMP_OUT() do {} while (0)

__global__ __launch_bounds__(512, 2) void fused_block(const char* __restrict__ x, const char* __restrict__ wqkv, const char* __restrict__ wo,
                                                      const char* __restrict__ wg, const char* __restrict__ wu, const char* __restrict__ wd,
                                                      char* __restrict__ out, char* ws) {
#if __HIP_DEVICE_COMPILE__
  const int tid = __builtin_amdgcn_workitem_id_x(), lane = tid & 63;
  const u32 w = __builtin_amdgcn_readfirstlane(tid >> 6);
  const u32 bid = __builtin_amdgcn_workgroup_id_x(), c = bid & 7, r = bid >> 3;
  const u32 sg = w >> 2, w3 = w & 3;
  const u32 li = lane & 15, g = lane >> 4, q4 = li >> 2, p4 = lane & 3;
  STAMP(0);

  const u32 hg = (c & 1) * 4 + (r & 3), pA = (c >> 1) * 8 + (r >> 2), rot = (ROT) & 7;
  const i32x4 rsX = mk_rsrc(x, 2048 * 1024), rsW = mk_rsrc(wqkv, 512 * 3072);
  const u32 l8 = lane >> 3, s8 = lane & 7;
  const int xr = (int)(pA * 64) - 16 + (int)(16 * w3 + l8);
  const u32 vx0 = opaque((u32)(xr < 0 ? 0 : xr) * 1024 + ((s8 ^ (l8 >> 1)) << 4));
  const u32 vx1 = opaque((u32)(xr + 8 < 0 ? 0 : xr + 8) * 1024 + ((s8 ^ (4 + (l8 >> 1))) << 4));
  const u32 vx2 = opaque((pA * 64 + 48 + 4 * w3 + (l8 & 3)) * 1024 + ((s8 ^ ((2 * w3 + ((l8 & 3) >> 1)) & 7)) << 4));
  const u32 vw0 = opaque((16 * w3 + l8) * 3072 + hg * 128 + ((s8 ^ (((l8 >> 1) & 1) << 1)) << 4));
  const u32 vw1 = opaque((16 * w3 + 8 + l8) * 3072 + hg * 128 + ((s8 ^ ((((l8 >> 1) & 1) | 2) << 1)) << 4));
  const u64 mask0 = w3 == 0 ? ~0xffull : ~0ull;
  auto slot_of = [](u32 i) { return A_RING + A_SLOT * ((i & 3) ^ ((i >> 1) & 1)); };
  auto piece = [&](u32 i, u32 k) {
    const u32 kc = (i + rot) & 7, sl = slot_of(i);
    if (k == 0) dma16m(rsX, vx0, kc * 128, sl + w3 * 2048, mask0);
    else if (k == 1) dma16(rsX, vx1, kc * 128, sl + w3 * 2048 + 1024);
    else if (k < 8) dma16(rsW, (k & 1) ? vw1 : vw0, kc * 64 * 3072 + ((k - 2) >> 1) * 1024, sl + A_WOFF + ((k - 2) >> 1) * 8192 + w3 * 2048 + (k & 1) * 1024);
    else dma16m(rsX, vx2, kc * 128, sl + 8192 + w3 * 512, 0xffffffffull);
  };
  if (sg) {
#pragma unroll
    for (u32 k = 0; k < 9; ++k) piece(0, k);
  }
  const u32 pB = 4 * c + (r >> 3), cb = r & 7, qB = 2 * pB + sg;
  const u32 RC = (c >> 1) * 2 + (r >> 4), slice = (c & 1) * 16 + (r & 15), qC = 8 * RC + 2 * w3 + sg;
  const i32x4 rsS = mk_rsrc(ws, WS_END);
  u32x2 ev;
  asm volatile("buffer_load_dwordx2 %0, %1, %2, 0 offen sc1" : "=&v"(ev)
               : "v"(WS_EPOCH + 8 * (p4 == 0 ? pA : (p4 == 1 ? (pA + 1) & 31 : (p4 == 2 ? pB : 4 * RC + w3)))), "s"(rsS) : "memory");
  u64 eB;
  if (w == 3) {
    u64 pc;
    asm volatile("s_getpc_b64 %0" : "=s"(pc));
    dma16m(mk_rsrc((const void*)(pc & ~127ull), CODE_BYTES), (r * 8 + (lane & 7)) * 128, 0, 0, 0xffull);
  }
  u32x4 xrow[2] = {};
  if (!sg)
    asm volatile("buffer_load_dwordx4 %0, %2, %3, 0 offen\n\tbuffer_load_dwordx4 %1, %2, %3, 0 offen offset:1024"
                 : "=&v"(xrow[0]), "=&v"(xrow[1]) : "v"((pA * 64 + hg * 8 + 2 * w3) * 1024 + lane * 16), "s"(rsX) : "memory");
  if (w == 0 && lane < 32) *(LAS u32x4*)(A_RING + A_SLOT * (lane >> 3) + (lane & 7) * 16) = u32x4{0x3f803f80u, 0x3f803f80u, 0x3f803f80u, 0x3f803f80u};
  if (w == 1) *(LAS u32*)(L_CTL + lane * 4) = 0;
  STAMP(12);

  const u32 sG = slice >> 3, sM = slice & 7, n3 = sG == 3 ? 4 : 6;
  const u32 U0 = 22 * sG + (sM < n3 ? 3 * sM : n3 + 2 * sM), nU = sM < n3 ? 3 : 2;
  u64 tagA, tagB, tagC;
  const __amdgpu_buffer_rsrc_t rWS = __builtin_amdgcn_make_buffer_rsrc(ws, 0, WS_END, 0x00020000);
  const i32x4 rsWo = mk_rsrc(wo, 512 * 1024), rsG = mk_rsrc(wg, 512 * 2752), rsU = mk_rsrc(wu, 512 * 2752), rsD = mk_rsrc(wd, 1376 * 1024);
  const u32 vsq = l8 * 1024 + cb * 128 + ((s8 ^ (((lane >> 4) & 3) << 1)) << 4);

  const u64 pT = w3 == 0 ? (u64)wg : (w3 == 1 ? (u64)wu : (w3 == 2 ? (u64)wd : (u64)wo));
  const i32x4 rsT = mk_rsrc((const void*)pT, w3 == 3 ? 512 * 1024 : 512 * 2752);
  const u32 nT = w3 == 3 ? 16u : 43u;
  const u32 vT = (u32)lane < nT ? (bid * nT + lane) * 128 : 0x80000000u;
  f32x4 aQ[2], aK[2], aV[2], aQ3, aK3, aV3;
  const bool par = li & 1;
  const u32 kA = 4 * g + par;
  const bool curA = kA <= li, curB = kA + 2 <= li;
  const u32 selA = par ? 0x01000c0cu : 0x0c0c0100u, selB = par ? 0x03020c0cu : 0x0c0c0302u, selZ = 0x0c0c0c0cu;
  const u32 sel0 = opaque(curA ? selZ : selA), sel1 = opaque(curB ? selZ : selB), sel2 = opaque(curA ? selA : selZ), sel3 = opaque(curB ? selB : selZ);
  f32x4 off0;
  for (int e = 0; e < 4; ++e) off0[e] = u2f(opaque(f2u((pA == 0 && sg == 0) ? -1e30f : 0.f)));
  const u64 maskL = opaque_s(w3 < 3 ? 0x0fffffffffffffffull : 0xfffull);
  const u32 gur = (u32)lane / 6, guc = lane - gur * 6;
  const u32 vgu = opaque(guc < 2 * nU ? (10 * w3 + gur) * 2752 + U0 * 32 + guc * 16 : 0x80000000u);
  auto rows10 = [&](i32x4 rs, u32 img, u32 j) {
    dma16m(rs, vgu, j * 40 * 2752, img + (4 * j + w3) * 960, j < 12 ? 0x0fffffffffffffffull : maskL);
  };
  u32x4 stg = {}, L[11];
  const u32 sto = WS_ST + (pA * 80 + 20 * w3 + (lane < 20 ? lane : 19)) * 16;
  {
    const u32 xa = li * 128 + (((g ^ (li >> 1)) & 7) << 4);
    const u32 xaF = xa ^ (sg << 6), xaO = xaF + (2 * sg + 1) * 2048, xaS = xaO ^ 64;
    const u32 wa = A_WOFF + (8 * g + q4) * 128 + (((2 * w3 + (p4 >> 1)) ^ (((q4 >> 1) | ((g & 1) << 1)) << 1)) << 4) + (p4 & 1) * 8;
    const u32 waF = wa + sg * 4096, waS = wa + (1 - sg) * 4096;
    u32 ab[2][5];
#pragma unroll
    for (u32 h = 0; h < 2; ++h) {
      const u32 sl = A_RING + 2 * h * A_SLOT;
      ab[h][0] = opaque(sl + xaF); ab[h][1] = opaque(sl + xaO); ab[h][2] = opaque(sl + xaS); ab[h][3] = opaque(sl + waF); ab[h][4] = opaque(sl + waS);
    }
    auto run = [&](auto role) {
    constexpr bool LD = role.value;
    if (LD) {
#pragma unroll
      for (u32 k = 0; k < 9; ++k) piece(1, k);
    }
    u32x4 F[2][11];
#pragma unroll
    for (u32 i = 0; i < 8; ++i) {
      u32x4 (&N)[11] = F[i & 1];
      u32x4 (&P)[11] = F[(i & 1) ^ 1];
      if (i < 8) {
        if (i < 7) STAMP(32 + i);
        if (LD) { if (i < 7) WAITV(9); else WAITV(8); } else if (i == 0) WAITV(0);
        BARRIER();
        if (i == 0) STAMP(13);
      }
      const u32 sn = (i & 3) ^ ((i >> 1) & 1), so = (sn & 1) * A_SLOT;
      const u32 x3a = ab[sn >> 1][0] + so, xoa = ab[sn >> 1][1] + so, xsa = ab[sn >> 1][2] + so, wfa = ab[sn >> 1][3] + so, wsa = ab[sn >> 1][4] + so;
      auto rd = [&](u32 k) {
        if (i >= 8) return;
        switch (k) {
          case 0: N[0] = trfrag(wfa, wfa + 512); break;
          case 1: N[1] = lds128(xoa); N[2] = lds128(xoa + 2048); break;
          case 2: N[3] = lds128(x3a); break;
          case 3: N[4] = trfrag(wfa + 8192, wfa + 8192 + 512); break;
          case 4: N[5] = trfrag(wfa + 16384, wfa + 16384 + 512); break;
          case 5: N[6] = trfrag(wsa, wsa + 512); break;
          case 6: N[7] = lds128(xsa); N[8] = lds128(xsa + 2048); break;
          case 7: N[9] = trfrag(wsa + 8192, wsa + 8192 + 512); break;
          case 8: N[10] = trfrag(wsa + 16384, wsa + 16384 + 512); break;
        }
      };
      auto cp = [&](u32 k) {
        if (i == 0) return;
        if (LD) {
          if (i < 6) piece(i + 2, k);
          else if (i < 8 && k < 8) { const u32 pc = ((i - 6) * 8 + k) * 4 + w3; dma16(rsWo, vsq, pc * 8192, pc * 1024); }
        } else {
          if (k == 0 && i == 4) ld_sc1(stg, rsS, sto);
        }
      };
      auto mm = [&](f32x4& acc, u32 a, u32 b, bool first) { if (i == 0) return; if (first && i == 1) mma0(acc, P[a], P[b]); else mma(acc, P[a], P[b]); };
      SCHED();
      mm(aQ[0], 0, 1, true); rd(0); SCHED();
      mm(aQ[1], 0, 2, true); rd(1); cp(0); SCHED();
      mm(aQ3, 0, 3, true); rd(2); SCHED();
      mm(aK[0], 4, 1, true); rd(3); cp(1); SCHED();
      mm(aK[1], 4, 2, true); rd(4); cp(2); SCHED();
      mm(aK3, 4, 3, true); rd(5); SCHED();
      mm(aV[0], 1, 5, true); rd(6); cp(3); SCHED();
      mm(aV[1], 2, 5, true); rd(7); cp(4); SCHED();
      mm(aV3, 3, 5, true); rd(8); SCHED();
      mm(aQ[0], 6, 7, false); cp(5); SCHED();
      mm(aQ[1], 6, 8, false); cp(6); SCHED();
      mm(aK[0], 9, 7, false); SCHED();
      mm(aK[1], 9, 8, false); cp(7); SCHED();
      mm(aV[0], 7, 10, false); cp(8); SCHED();
      mm(aV[1], 8, 10, false); SCHED();
      if (i == 0 && LD) {
#pragma unroll
        for (u32 k = 0; k < 9; ++k) piece(2, k);
      }
      if (i == (LD ? 1 : 0)) {
        asm volatile("" : "+v"(ev));
        eB = epoch(ev, 2);
        tagA = opaque_s(mk_tag(epoch(ev, 0))); tagB = opaque_s(mk_tag(eB)); tagC = opaque_s(mk_tag(epoch(ev, 3)));
        SCHED();
      }
      if (i == 0 && !LD) {
        asm volatile("" : "+v"(xrow[0]), "+v"(xrow[1]));
        const u32 tN = (u32)mk_tag(epoch(ev, 1));
        float s[2] = {0.f, 0.f}, q[2] = {0.f, 0.f};
#pragma unroll
        for (u32 k = 0; k < 8; ++k) { s[k & 1] = dot2(xrow[k & 1][k >> 1], 0x3f803f80u, s[k & 1]); q[k & 1] = dot2(xrow[k & 1][k >> 1], xrow[k & 1][k >> 1], q[k & 1]); }
        const auto hs = __builtin_amdgcn_permlane32_swap(f2u(s[0]), f2u(s[1]), false, false), hq = __builtin_amdgcn_permlane32_swap(f2u(q[0]), f2u(q[1]), false, false);
        float ss = u2f(hs[0]) + u2f(hs[1]), qq = u2f(hq[0]) + u2f(hq[1]);
        ss += dpp<0xB1>(ss); qq += dpp<0xB1>(qq); ss += dpp<0x4E>(ss); qq += dpp<0x4E>(qq);
        ss += dpp<0x141>(ss); qq += dpp<0x141>(qq); ss += dpp<0x140>(ss); qq += dpp<0x140>(qq);
        const auto gs = __builtin_amdgcn_permlane16_swap(f2u(ss), f2u(ss), false, false), gq = __builtin_amdgcn_permlane16_swap(f2u(qq), f2u(qq), false, false);
        ss = u2f(gs[0]) + u2f(gs[1]); qq = u2f(gq[0]) + u2f(gq[1]);
        const float m = ss * (1.f / 512), rs = __builtin_amdgcn_rsqf(fmx(qq * (1.f / 512) - m * m, 0.f) + LN_EPS);
        const u32 j = hg * 8 + 2 * w3 + (lane >> 5);
        const u32 tg = (lane & 1) ? tN : (u32)tagA;
        const u32 o = (lane & 31) == 0 ? (pA * 80 + 16 + j) * 16 : ((lane & 31) == 1 && j >= 48 && pA < 31 ? ((pA + 1) * 80 + j - 48) * 16 : 0x80000000u);
        __builtin_amdgcn_raw_buffer_store_b128(u32x4{f2u(m), tg, f2u(rs), tg}, rWS, WS_ST + o, 0, AUX_SC1);
        SCHED();
      }
      if (i == 7 && !LD) {
        asm volatile("s_waitcnt vmcnt(0)" : "+v"(stg)::"memory");
        const bool none = pA == 0 && 20 * w3 + (lane < 20 ? lane : 19) < 16;
#pragma unroll 1
        for (u32 s = 0; s < SPIN_CAP; ++s) {
          if (__builtin_amdgcn_ballot_w64(!none && (stg.y != (u32)tagA || stg.w != (u32)tagA)) == 0) break;
          stg = __builtin_amdgcn_raw_buffer_load_b128(rWS, opaque(sto), 0, AUX_SC1);
        }
        if (lane < 20) *(LAS f32x2*)(A_SCR_ST + (20 * w3 + lane) * 8) = none ? f32x2{0.f, 1.f} : f32x2{u2f(stg.x), u2f(stg.z)};
        dma4(rsT, vT, L_SINK);
        SCHED();
      }
    }
#pragma unroll
    for (u32 k = 0; k < 11; ++k) L[k] = F[1][k];
    };
    if (sg) { __builtin_amdgcn_s_setprio(1); run(On{}); __builtin_amdgcn_s_setprio(0); } else run(Off{});
  }
  STAMP(39);
  {
    mma(aK3, L[4], L[3]); mma(aV3, L[3], L[5]); mma(aQ3, L[0], L[3]); mma(aK[1], L[4], L[2]); mma(aV[1], L[2], L[5]); mma(aK[1], L[9], L[8]); mma(aV[1], L[8], L[10]);
    mma(aK[0], L[4], L[1]); mma(aK[0], L[9], L[7]); mma(aV[0], L[1], L[5]);
    asm volatile("s_nop 5" : "+v"(aK3), "+v"(aV3), "+v"(aQ3), "+v"(aK[1]), "+v"(aV[1]));
    if (sg) { *(LAS f32x4*)(A_SCR + w * 2048 + lane * 16) = aK3; *(LAS f32x4*)(A_SCR + w * 2048 + 1024 + lane * 16) = aV3; }
    else { *(LAS f32x4*)(A_SCR + w * 2048 + lane * 16) = aK[1]; *(LAS f32x4*)(A_SCR + w * 2048 + 1024 + lane * 16) = aV[1]; }
    {
      const u32 a = A_SCR_CS + w * 192 + g * 16;
      asm volatile("s_mov_b64 exec, %4\n\tds_write_b128 %0, %1\n\tds_write_b128 %0, %2 offset:64\n\ts_mov_b64 exec, 0xffff\n\tds_write_b32 %3, %5\n\ts_mov_b64 exec, -1"
                   ::"v"(a), "v"(aQ3), "v"(aK3), "v"(A_SCR_CS + w * 192 + 128 + lane * 4), "s"(0x0001000100010001ull), "v"(aV3[0]) : "memory");
    }
    STAMP(14);
    asm volatile("s_waitcnt lgkmcnt(0)\n\ts_barrier" : "+v"(aK[0])::"memory");
    STAMP(15);
    const u32 pw = w ^ 4;
    const u32 aS = opaque(A_SCR + pw * 2048 + lane * 16), aC = opaque(A_SCR_CS + w3 * 192 + g * 16), aCv = opaque(A_SCR_CS + w3 * 192 + 128 + li * 4);
    const u32 aT = opaque(A_SCR_ST + sg * 256 + li * 8), aTv = opaque(A_SCR_ST + sg * 256 + g * 32);
    const f32x4 ck0 = lds128f(aC + 64), ck1 = lds128f(aC + 768 + 64);
    f32x2 sT[3];
    f32x4 t0[3], t1[3];
#pragma unroll
    for (int b = 2; b >= 0; --b) sT[b] = *(const LAS f32x2*)(aT + b * 128);
    const f32x4 pK = lds128f(aS), cq0 = lds128f(aC), cq1 = lds128f(aC + 768);
    const float cv0 = *(const LAS float*)aCv, cv1 = *(const LAS float*)(aCv + 768);
#pragma unroll
    for (int b = 2; b >= 0; --b) { t0[b] = lds128f(aTv + b * 128); t1[b] = lds128f(aTv + b * 128 + 16); }
    const f32x4 pV = lds128f(aS + 1024);
    arrive(C_AS);
    SCHED();
    mma(aQ[0], L[0], L[1]); mma(aQ[1], L[0], L[2]); mma(aQ[0], L[6], L[7]); mma(aQ[1], L[6], L[8]); mma(aV[0], L[7], L[10]);
    SCHED();
    if (sg) {
#pragma unroll
      for (u32 j = 1; j <= EARLY; ++j) rows10(rsG, L_WG, j);
    }
    SCHED();
    const float own3 = sg ? 0.f : 1.f;
    f32x4 csq, csk;
    u32x2 Kp[3], Vp[3], Qp[2];
    float rq[2];
#pragma unroll
    for (int k = 0; k < 4; ++k) csk[k] = ck0[k] + ck1[k];
#pragma unroll
    for (int b = 2; b >= 0; --b) {
      f32x4 k;
      if (b == 0) {
#pragma unroll
        for (int e = 0; e < 4; ++e) k[e] = __builtin_fmaf(own3, aK3[e], pK[e]);
      } else k = aK[b - 1];
      const f32x2 s = sT[b];
      Kp[b] = u32x2{pk((k[0] - s.x * csk[0]) * s.y, (k[1] - s.x * csk[1]) * s.y), pk((k[2] - s.x * csk[2]) * s.y, (k[3] - s.x * csk[3]) * s.y)};
    }
#pragma unroll
    for (int k = 0; k < 4; ++k) csq[k] = cq0[k] + cq1[k];
    asm volatile("" : "+v"(Kp[0]), "+v"(Kp[1]), "+v"(Kp[2]), "+v"(csq), "+v"(aQ[0]), "+v"(aQ[1]), "+v"(aV[0]));
#pragma unroll
    for (int b = 0; b < 2; ++b) {
      const f32x4 q = aQ[b];
      const f32x2 s = sT[b + 1];
      Qp[b] = u32x2{pk(q[0] - s.x * csq[0], q[1] - s.x * csq[1]), pk(q[2] - s.x * csq[2], q[3] - s.x * csq[3])};
      rq[b] = s.y * (0.25f * LOG2E);
    }
    STAMP(16);
    const f32x4 z = {0.f, 0.f, 0.f, 0.f};
    f32x4 sc[2], sp[2];
#pragma unroll
    for (int b = 0; b < 2; ++b) { sc[b] = mfma16(Kp[b + 1], Qp[b], z); sp[b] = mfma16(Kp[b], Qp[b], b == 0 ? off0 : z); }
    const float csv = cv0 + cv1;
#pragma unroll
    for (int b = 2; b >= 0; --b) {
      f32x4 v;
      if (b == 0) {
#pragma unroll
        for (int e = 0; e < 4; ++e) v[e] = __builtin_fmaf(own3, aV3[e], pV[e]);
      } else v = aV[b - 1];
      Vp[b] = u32x2{pk((v[0] - t0[b][0] * csv) * t0[b][1], (v[1] - t0[b][2] * csv) * t0[b][3]),
                    pk((v[2] - t1[b][0] * csv) * t1[b][1], (v[3] - t1[b][2] * csv) * t1[b][3])};
    }
    float la[2], lb[2], mx[2], ea[2], eb[2], inv[2];
#pragma unroll
    for (int b = 0; b < 2; ++b) {
      la[b] = sel(curA, sel(par, sc[b][1], sc[b][0]), sel(par, sp[b][1], sp[b][0]));
      lb[b] = sel(curB, sel(par, sc[b][3], sc[b][2]), sel(par, sp[b][3], sp[b][2]));
    }
#pragma unroll
    for (int b = 0; b < 2; ++b) mx[b] = xmax4(fmx(la[b], lb[b]));
#pragma unroll
    for (int b = 0; b < 2; ++b) { ea[b] = __builtin_amdgcn_exp2f((la[b] - mx[b]) * rq[b]); eb[b] = __builtin_amdgcn_exp2f((lb[b] - mx[b]) * rq[b]); }
#pragma unroll
    for (int b = 0; b < 2; ++b) inv[b] = __builtin_amdgcn_rcpf(xsum4(ea[b] + eb[b]));
    u32x2 Op[2];
#pragma unroll
    for (int b = 0; b < 2; ++b) {
      const u32 pp = pk(ea[b] * inv[b], eb[b] * inv[b]);
      const u32x4 P = {__builtin_amdgcn_perm(pp, pp, sel0), __builtin_amdgcn_perm(pp, pp, sel1), __builtin_amdgcn_perm(pp, pp, sel2), __builtin_amdgcn_perm(pp, pp, sel3)};
      const f32x4 o = mfma32(u32x4{Vp[b].x, Vp[b].y, Vp[b + 1].x, Vp[b + 1].y}, P, z);
      Op[b] = u32x2{pk(o[0], o[1]), pk(o[2], o[3])};
    }
    const auto s0 = __builtin_amdgcn_permlane32_swap(Op[0].x, Op[1].x, false, false);
    const auto s1 = __builtin_amdgcn_permlane32_swap(Op[0].y, Op[1].y, false, false);
    const u32 H = 4 * hg + w3;
    __builtin_amdgcn_raw_buffer_store_b128(u32x4{s0[0], s1[0], s0[1], s1[1]}, rWS,
        WS_O + ((4 * pA + 2 * sg + (lane >> 5)) * 16 + (H >> 1)) * 1024 + (H & 1) * 512 + (lane & 31) * 16, 0, AUX_SC1);
    SCHED();
  }
  STAMP(17);

  const u32 bF = w3 >> 1, sF = w3 & 1;
  u32 sqa[4];
  {
    const u32 a = (16 * (g >> 1) + 4 * (g & 1) + q4) * 128 + ((((2 * (g & 1) + (q4 >> 1)) << 1) + (p4 >> 1)) << 4) + (p4 & 1) * 8;
#pragma unroll
    for (u32 u = 0; u < 4; ++u) sqa[u] = a ^ ((u ^ (2 * sF)) << 5);
  }
  u32x2 hp[2];
  {
    u32x4 fw[8][4];
    WAITV(0);
    if (lane == 0) signal(ws, WS_FA + ((2 * pA + sg) * 4 + (hg >> 1)) * 128 + ((hg & 1) * 4 + w3) * 8, tagA);
    STAMP(2);
    if (sg) {
      arrive(C_WL);
#pragma unroll
      for (u32 j = EARLY + 1; j < 13; ++j) rows10(rsG, L_WG, j);
      await(C_AS, 8);
#pragma unroll
      for (u32 j = 0; j < 13; ++j) rows10(rsU, L_WU, j);
    }
    await(C_WL, 4);
#pragma unroll
    for (u32 u = 0; u < 4; ++u) {
      const u32 a = opaque(sqa[u] + sF * 32768);
#pragma unroll
      for (u32 s = 0; s < 8; ++s) fw[s][u] = trfrag(a + s * 4096, a + s * 4096 + 1024);
    }
    arrive(C_WO);
    const __amdgpu_buffer_rsrc_t rX = __builtin_amdgcn_make_buffer_rsrc((void*)x, 0, 2048 * 1024, 0x00020000);
    const u32 xoff = (32 * qB + 16 * bF + li) * 1024 + cb * 128 + sF * 64 + g * 8;
    const u32x2 xr0 = __builtin_amdgcn_raw_buffer_load_b64(rX, xoff, 0, 0), xr1 = __builtin_amdgcn_raw_buffer_load_b64(rX, xoff + 32, 0, 0);
    if (sg) {
      await(C_WO, 8);
      rows10(rsG, L_WG, 0);
      WAITV(0);
      arrive(C_WGU);
    }
    STAMP(20);
    wait_tags(ws, WS_FA + (qB * 4 + 2 * sF + (l8 & 1)) * 128 + s8 * 8, tagB);
    STAMP(3);
    u32x4 fa[8];
#pragma unroll
    for (u32 s = 0; s < 8; ++s) fa[s] = __builtin_amdgcn_raw_buffer_load_b128(rWS, WS_O + lane * 16, ((2 * qB + bF) * 16 + 8 * sF + s) * 1024, AUX_SC1);
    await(C_WO, 8);
    SCHED();
    f32x4 acc[4];
#pragma unroll
    for (u32 s = 0; s < 8; ++s)
#pragma unroll
      for (u32 u = 0; u < 4; ++u) { if (s == 0) mma0(acc[3 - u], fw[s][3 - u], fa[s]); else mma(acc[3 - u], fw[s][3 - u], fa[s]); }
    MMA_DONE();
    STAMP(21);
    *(LAS f32x4*)((w ^ 1) * 2048 + lane * 16) = acc[2];
    *(LAS f32x4*)((w ^ 1) * 2048 + 1024 + lane * 16) = acc[3];
    arrive(C_B2 + (w >> 1));
    await(C_B2 + (w >> 1), 2);
    STAMP(22);
    const f32x4 o0 = lds128f(w * 2048 + lane * 16), o1 = lds128f(w * 2048 + 1024 + lane * 16);
    arrive(C_BS);
    hp[0] = u32x2{pk(acc[0][0] + bflo(xr0.x) + o0[0], acc[0][1] + bfhi(xr0.x) + o0[1]), pk(acc[0][2] + bflo(xr0.y) + o0[2], acc[0][3] + bfhi(xr0.y) + o0[3])};
    hp[1] = u32x2{pk(acc[1][0] + bflo(xr1.x) + o1[0], acc[1][1] + bfhi(xr1.x) + o1[1]), pk(acc[1][2] + bflo(xr1.y) + o1[2], acc[1][3] + bfhi(xr1.y) + o1[3])};
    __builtin_amdgcn_raw_buffer_store_b128(u32x4{hp[0].x, hp[0].y, hp[1].x, hp[1].y}, rWS, WS_H + lane * 16 + ((2 * qB + bF) * 16 + 2 * cb + sF) * 1024, 0, AUX_SC1);
    float s = dot2(hp[0].x, 0x3f803f80u, 0.f), qq = dot2(hp[0].x, hp[0].x, 0.f);
    s = dot2(hp[0].y, 0x3f803f80u, s); qq = dot2(hp[0].y, hp[0].y, qq);
    s = dot2(hp[1].x, 0x3f803f80u, s); qq = dot2(hp[1].x, hp[1].x, qq);
    s = dot2(hp[1].y, 0x3f803f80u, s); qq = dot2(hp[1].y, hp[1].y, qq);
    s = xsum4(s); qq = xsum4(qq);
    if (lane < 16)
      __builtin_amdgcn_raw_buffer_store_b64(u32x2{f2u(s), f2u(qq)}, rWS, WS_HST + lane * 8 + ((2 * qB + bF) * 16 + 2 * cb + sF) * 128, 0, AUX_SC1);
    STAMP(23);
    WAITV(0);
    if (arrive_old(C_B3 + sg) == 3 && lane == 0) signal(ws, WS_FB + qB * 128 + cb * 8, tagB);
  }
  STAMP(4);

  if (!sg) {
    await(C_WGU, 4);
    const u32x4 ones = {opaque(0x3f803f80u), opaque(0x3f803f80u), opaque(0x3f803f80u), opaque(0x3f803f80u)};
    const u32 ca = (4 * g + q4) * 96 + p4 * 8;
    const u32 u1 = w3 < 2 ? 4 + w3 : w3;
    const u32 a0 = opaque((w3 < 3 ? L_WG + w3 * 32 : L_WU) + ca), a1 = opaque((u1 < 3 ? L_WG + u1 * 32 : L_WU + (u1 - 3) * 32) + ca);
    f32x4 cs0, cs1;
    u32x4 f0[16], f1[16];
#pragma unroll
    for (u32 s = 0; s < 16; ++s) { f0[s] = trfrag(a0 + s * 3072, a0 + s * 3072 + 1536); f1[s] = trfrag(a1 + s * 3072, a1 + s * 3072 + 1536); }
    SCHED();
#pragma unroll
    for (u32 s = 0; s < 16; ++s) { if (s == 0) { mma0(cs0, f0[s], ones); mma0(cs1, f1[s], ones); } else { mma(cs0, f0[s], ones); mma(cs1, f1[s], ones); } }
    MMA_DONE();
    if (li == 0) { *(LAS f32x4*)(L_CS + w3 * 64 + g * 16) = cs0; *(LAS f32x4*)(L_CS + u1 * 64 + g * 16) = cs1; }
    arrive(C_CS);
  }
  STAMP(5);

  u32x4 fd[3][4];
  {
    await(C_WGU, 4);
    const u32 aG = opaque(L_WG + (4 * g + q4) * 96 + p4 * 8), aU = opaque(L_WU + (4 * g + q4) * 96 + p4 * 8);
    u32x4 fg[3], fu[3];
#pragma unroll
    for (u32 u = 0; u < 3; ++u) { fg[u] = trfrag(aG + u * 32, aG + 1536 + u * 32); fu[u] = trfrag(aU + u * 32, aU + 1536 + u * 32); }
    wait_tags(ws, WS_FB + qC * 128 + s8 * 8, tagC);
    STAMP(6);
    constexpr u32 WIN = 8;
    u32x4 fa[16][2];
    auto ldA = [&](u32 s, u32 b) { fa[s][b] = __builtin_amdgcn_raw_buffer_load_b128(rWS, WS_H + lane * 16, ((2 * qC + b) * 16 + s) * 1024, AUX_SC1); };
#pragma unroll
    for (u32 s = 0; s < 4; ++s) { ldA(s, 0); ldA(s, 1); }
    SCHED();
    f32x4 cg[2][3], cu[2][3];
    u32x2 sp[2][4];
#pragma unroll
    for (u32 s = 0; s < 16; ++s) {
      if (s == 4) await(C_BS, 8);
#pragma unroll
      for (u32 u = 0; u < 3; ++u) {
        const u32 o = (s + 1) * 3072 + u * 32;
        if (s == 0) { mma0(cg[0][u], fg[u], fa[s][0]); mma0(cg[1][u], fg[u], fa[s][1]); } else { mma(cg[0][u], fg[u], fa[s][0]); mma(cg[1][u], fg[u], fa[s][1]); }
        SCHED();
        if (s < 15) fg[u] = trfrag(aG + o, aG + o + 1536);
        if (u < 2 && s < 4) ldA(4 + 2 * s, u);
        SCHED();
        if (s == 0) { mma0(cu[0][u], fu[u], fa[s][0]); mma0(cu[1][u], fu[u], fa[s][1]); } else { mma(cu[0][u], fu[u], fa[s][0]); mma(cu[1][u], fu[u], fa[s][1]); }
        SCHED();
        if (s < 15) fu[u] = trfrag(aU + o, aU + o + 1536);
        if (u < 2 && s < 4) ldA(5 + 2 * s, u);
        if (u < 2 && s >= 4 && s + WIN < 16) ldA(s + WIN, u);
        if (u < 2 && s >= 8 && s < 12) sp[u][s - 8] = __builtin_amdgcn_raw_buffer_load_b64(rWS, WS_HST + (4 * g + s - 8) * 128 + li * 8, (2 * qC + u) * 2048, AUX_SC1);
        if (u == 2 && s >= 4 && s < 12) {
          const u32 k = s - 4, pc = k & 3;
          if (k < 4) dma16(rsD, vsq, ((11 * w3 + 2 * sg) * 4 + pc) * 8192, ((8 * sg + w3) * 4 + pc) * 1024);
          else {
            const u32 d = ((4 + 8 * sg + w3) * 4 + pc) * 1024;
            dma16(rsD, vsq, ((11 * w3 + 1 + 9 * sg) * 4 + pc) * 8192, d < L_WG - 1024 ? d : L_WG - 1024);
          }
        }
        if (s == 14 && u == 2) arrive(C_CD);
        SCHED();
      }
    }
    WAITV(0);
    arrive(C_P1);
    STAMP(7);
    await(C_CS, 4);
    f32x4 cs[6];
#pragma unroll
    for (u32 u = 0; u < 6; ++u) cs[u] = lds128f(L_CS + u * 64 + g * 16);
    SCHED();
    constexpr float C1 = -2.f * 0.7978845608028654f * LOG2E, C2 = C1 * 0.044715f;
    float mean[2], k1[2], k2[2], kv[2];
#pragma unroll
    for (u32 b = 0; b < 2; ++b) {
      float s = (u2f(sp[b][0].x) + u2f(sp[b][1].x)) + (u2f(sp[b][2].x) + u2f(sp[b][3].x));
      float qq = (u2f(sp[b][0].y) + u2f(sp[b][1].y)) + (u2f(sp[b][2].y) + u2f(sp[b][3].y));
      s = xsum4(s); qq = xsum4(qq);
      mean[b] = s * (1.f / 512);
      kv[b] = fmx(qq * (1.f / 512) - mean[b] * mean[b], 0.f) + LN_EPS;
      const float rs = __builtin_amdgcn_rsqf(kv[b]);
      k1[b] = C1 * rs; k2[b] = C2 * rs * rs * rs;
    }
#pragma unroll
    for (u32 u = 0; u < 3; ++u) {
      u32x2 o[2];
#pragma unroll
      for (u32 b = 0; b < 2; ++b) {
        float v[4];
#pragma unroll
        for (u32 k = 0; k < 4; ++k) {
          const float G = cg[b][u][k] - mean[b] * cs[u][k], U = cu[b][u][k] - mean[b] * cs[3 + u][k];
          const float e = __builtin_amdgcn_exp2f(G * __builtin_fmaf(G * G, k2[b], k1[b]));
          v[k] = G * U * __builtin_amdgcn_rcpf(__builtin_fmaf(e, kv[b], kv[b]));
        }
        o[b] = u32x2{pk(v[0], v[1]), pk(v[2], v[3])};
      }
      const auto s0 = __builtin_amdgcn_permlane32_swap(o[0].x, o[1].x, false, false);
      const auto s1 = __builtin_amdgcn_permlane32_swap(o[0].y, o[1].y, false, false);
      const u32 U = U0 + u;
      __builtin_amdgcn_raw_buffer_store_b128(u32x4{s0[0], s1[0], s0[1], s1[1]}, rWS,
          u < nU ? WS_HH + (lane >> 5) * 43 * 1024 + (lane & 31) * 16 + (2 * qC * 43 + (U >> 1)) * 1024 + (U & 1) * 512 : 0x80000000u, 0, AUX_SC1);
    }
    STAMP(26);
    WAITV(0);
    if (lane == 0) signal(ws, WS_FC + (qC * 4 + sG) * 128 + sM * 8, tagC);
    STAMP(8);
    await(C_P1, 8);
#pragma unroll
    for (u32 u = 0; u < 4; ++u) {
      const u32 a = opaque(sqa[u] + w3 * 4096);
#pragma unroll
      for (u32 i = 0; i < 3; ++i) fd[i][u] = trfrag(a + i * 16384, a + i * 16384 + 1024);
    }
    arrive(C_PULL);
  }

  {
    if (!sg) {
      await(C_CD, 8);
#pragma unroll
      for (u32 k = 0; k < 24; ++k) dma16(rsD, vsq, ((11 * w3 + 3 + (k >> 2)) * 4 + (k & 3)) * 8192, L_WG + ((4 * (k >> 2) + w3) * 4 + (k & 3)) * 1024);
      WAITV(0);
      arrive(C_P2);
    }
    STAMP(27);
    wait_tags(ws, WS_FC + (qB * 4 + w3) * 128 + s8 * 8, tagB);
    STAMP(9);
    u32x4 fa[11][2];
    auto ldA = [&](u32 i, u32 b) {
      const u32 ks = (i == 10 && w3 == 3) ? 42 : 11 * w3 + i;
      fa[i][b] = __builtin_amdgcn_raw_buffer_load_b128(rWS, WS_HH + lane * 16, ((2 * qB + (b ^ bF)) * 43 + ks) * 1024, AUX_SC1);
    };
#pragma unroll
    for (u32 i = 0; i < 5; ++i) { ldA(i, 0); ldA(i, 1); }
    await(C_P2, 4);
    STAMP(28);
    f32x4 acc[2][4];
    u32 da[4], db[4], dc[4];
#pragma unroll
    for (u32 u = 0; u < 4; ++u) { da[u] = opaque(sqa[u] + w3 * 4096); db[u] = opaque(sqa[u] + w3 * 4096 + L_WG); dc[u] = opaque(sqa[u] + w3 * 4096 + L_WG + 49152); }
    auto ldW = [&](u32 i, u32 u) {
      const u32 a = i < 6 ? db[u] + (i - 3) * 16384 : (i < 9 ? dc[u] + (i - 6) * 16384 : da[u] + (i - 9) * 49152);
      return trfrag(a, a + 1024);
    };
    u32x4 fl[4];
#pragma unroll
    for (u32 u = 0; u < 4; ++u) fl[u] = ldW(3, u);
    SCHED();
#pragma unroll
    for (u32 i = 0; i < 11; ++i) {
      if (i == 3 && !sg) {
        await(C_PULL, 8);
#pragma unroll
        for (u32 k = 0; k < 4; ++k) dma16(rsD, vsq, ((11 * w3 + 9) * 4 + k) * 8192, (w3 * 4 + k) * 1024);
      }
      if (i == 8) {
        if (!sg) { WAITV(0); arrive(C_PB); }
        await(C_PB, 4);
      }
      if (i < 10 || w3 < 3) {
#pragma unroll
        for (u32 u = 0; u < 4; ++u) {
          const u32x4 f = i < 3 ? fd[i][u] : fl[u];
          if (i == 0) { mma0(acc[0][u], f, fa[i][0]); mma0(acc[1][u], f, fa[i][1]); } else { mma(acc[0][u], f, fa[i][0]); mma(acc[1][u], f, fa[i][1]); }
          SCHED();
          if (i >= 3 && i < 10) fl[u] = ldW(i + 1, u);
          if (i < 3) ldA(5 + 2 * i + (u >> 1), u & 1);
          SCHED();
        }
      }
    }
    MMA_DONE();
    STAMP(10);
    if (sg) await(C_D0, 4);
#pragma unroll
    for (u32 d = 1; d < 4; ++d) {
      const u32 a = D_SCR + ((w3 ^ d) * 3 + d - 1) * 2048 + lane * 16;
      *(LAS f32x4*)a = acc[d >> 1][(d & 1) * 2];
      *(LAS f32x4*)(a + 1024) = acc[d >> 1][(d & 1) * 2 + 1];
    }
    arrive(C_D2 + sg);
    await(C_D2 + sg, 4);
    f32x4 p0[3], p1[3];
#pragma unroll
    for (u32 d = 0; d < 3; ++d) { const u32 a = D_SCR + (w3 * 3 + d) * 2048 + lane * 16; p0[d] = lds128f(a); p1[d] = lds128f(a + 1024); }
    if (!sg) arrive(C_D0);
    float h0[4], h1[4];
#pragma unroll
    for (u32 k = 0; k < 4; ++k) { h0[k] = (acc[0][0][k] + p0[0][k]) + (p0[1][k] + p0[2][k]); h1[k] = (acc[0][1][k] + p1[0][k]) + (p1[1][k] + p1[2][k]); }
    const u32 x0 = pk(h0[0] + bflo(hp[0].x), h0[1] + bfhi(hp[0].x)), x1 = pk(h0[2] + bflo(hp[0].y), h0[3] + bfhi(hp[0].y));
    const u32 y0 = pk(h1[0] + bflo(hp[1].x), h1[1] + bfhi(hp[1].x)), y1 = pk(h1[2] + bflo(hp[1].y), h1[3] + bfhi(hp[1].y));
    const auto s0 = __builtin_amdgcn_permlane16_swap(x0, y0, false, false);
    const auto s1 = __builtin_amdgcn_permlane16_swap(x1, y1, false, false);
    *(GAS u32x4*)(out + (size_t)((32 * qB + 16 * bF + li) * 1024 + cb * 128 + sF * 64 + (g & 1) * 32 + (g >> 1) * 16)) = u32x4{s0[0], s1[0], s0[1], s1[1]};
    if (cb == 0 && tid == 256) signal(ws, WS_EPOCH + pB * 8, eB + 1);
    STAMP(11);
    STAMP_OUT();
  }
#endif
}

#if __HIP_DEVICE_COMPILE__
#pragma clang attribute pop
#endif

extern "C" void kernel_launch(void* const* d_in, const int* in_sizes, int n_in, void* d_out, int out_size, void* d_ws, size_t ws_size,
                              hipStream_t stream) {
  (void)hipFuncSetAttribute((const void*)fused_block, hipFuncAttributeMaxDynamicSharedMemorySize, LDS_BYTES);
  fused_block<<<256, 512, LDS_BYTES, stream>>>((const char*)d_in[0], (const char*)d_in[1], (const char*)d_in[2], (const char*)d_in[4],
                                               (const char*)d_in[5], (const char*)d_in[6], (char*)d_out, (char*)d_ws);
}
```
